```python
import torch

def _softmax(x, axis=-1):
    return torch.softmax(x, dim=axis)
def _gelu(x):
    return torch.nn.functional.gelu(x, approximate="tanh")
def _silu(x):
    return torch.nn.functional.silu(x)
def _sigmoid(x):
    return torch.sigmoid(x)
def _layernorm(x, axis=-1, eps=1e-5):
    m = x.mean(dim=axis, keepdim=True)
    v = x.var(dim=axis, keepdim=True, unbiased=False)
    return (x - m) / torch.sqrt(v + eps)
def _scatter_add(tgt, idx, src, axis=0):
    return tgt.clone().index_add_(axis, idx, src)
def _segment_sum(src, seg, n):
    out = torch.zeros((n,) + src.shape[1:], dtype=src.dtype)
    return out.index_add_(0, seg, src)
def _pad(x, pw):
    flat = tuple(v for pair in reversed(pw) for v in pair)
    return torch.nn.functional.pad(x, flat)

kernel_name = 'standalone__low_rank_proj_linformer__sampling'

def setup_inputs():

    _w = (lambda a: (lambda t: t.to(torch.bfloat16) if t.is_floating_point() else t)(torch.as_tensor(a)))
    B, H, Sq, Skv, Dh, R = 8, 32, 4, 2048, 128, 64
    return {
        "q": _w((lambda *s: torch.randn(*s, dtype=torch.bfloat16) * 0.8)(B, H, Sq, Dh)),
        "k": _w((lambda *s: torch.randn(*s, dtype=torch.bfloat16) * 0.8)(B, H, Skv, Dh)),
        "v": _w((lambda *s: torch.randn(*s, dtype=torch.bfloat16) * 0.05)(B, H, Skv, Dh)),
        "e_proj": _w((lambda *s: torch.randn(*s, dtype=torch.bfloat16) * 0.8)(R, Skv) * (Skv ** -0.5)),
        "f_proj": _w((lambda *s: torch.randn(*s, dtype=torch.bfloat16) * 0.05)(R, Skv) * (Skv ** -0.5)),
    }

def reference(q, k, v, e_proj, f_proj):
    B, H, Sq, Dh = q.shape
    kp = torch.einsum("rn,bhnd->bhrd", e_proj, k)
    vp = torch.einsum("rn,bhnd->bhrd", f_proj, v)
    scores = torch.einsum("bhqd,bhrd->bhqr", q, kp) * (Dh ** -0.5)
    p = _softmax(scores, axis=-1)
    o = torch.einsum("bhqr,bhrd->bhqd", p, vp)
    return o
```

```cpp
#include <hip/hip_runtime.h>
#include <hip/hip_bf16.h>
#include <stdint.h>

#define AUXKV 2
#define KLO 1
#define PRETOUCH 0
#define DELAY 0
#define STAMP 0

namespace lin {

constexpr int NW = 4;
constexpr int SQ = 4, SKV = 2048, DH = 128, RR = 64;
constexpr int NHEAD = 256;
constexpr int NIT = SKV / 64 / NW;

#define GAS __attribute__((address_space(1)))
#define LAS __attribute__((address_space(3)))
typedef unsigned u32x4 __attribute__((ext_vector_type(4)));
typedef unsigned u32x2 __attribute__((ext_vector_type(2)));
typedef float f32x4 __attribute__((ext_vector_type(4)));
typedef __bf16 bf16x8 __attribute__((ext_vector_type(8)));
typedef __bf16 bf16x2 __attribute__((ext_vector_type(2)));
typedef float f32x2 __attribute__((ext_vector_type(2)));
typedef __amdgpu_buffer_rsrc_t rsrc_t;

template <int AUX> __device__ __forceinline__ u32x4 ldb(rsrc_t r, unsigned voff, unsigned soff) {
  return __builtin_amdgcn_raw_buffer_load_b128(r, voff, soff, AUX);
}
__device__ __forceinline__ rsrc_t mkrsrc(const char* p, unsigned bytes) {
  return __builtin_amdgcn_make_buffer_rsrc((void*)p, 0, bytes, 0x00020000);
}
__device__ __forceinline__ f32x4 mfma(u32x4 a, u32x4 b, f32x4 c) {
  return __builtin_amdgcn_mfma_f32_16x16x32_bf16(__builtin_bit_cast(bf16x8, a), __builtin_bit_cast(bf16x8, b), c, 0, 0, 0);
}
__device__ __forceinline__ unsigned cvtpk(float lo, float hi) {
  f32x2 v = {lo, hi};
  bf16x2 r = __builtin_convertvector(v, bf16x2);
  return __builtin_bit_cast(unsigned, r);
}
__device__ __forceinline__ float bflo(unsigned u) { return __builtin_bit_cast(float, u << 16); }
__device__ __forceinline__ float bfhi(unsigned u) { return __builtin_bit_cast(float, u & 0xffff0000u); }

template <int CTRL> __device__ __forceinline__ float dpp(float x) {
  return __builtin_bit_cast(float, __builtin_amdgcn_mov_dpp(__builtin_bit_cast(int, x), CTRL, 0xf, 0xf, true));
}
constexpr int XOR1 = 0xB1, XOR2 = 0x4E, XOR7 = 0x141, XOR8 = 0x128;
__device__ __forceinline__ float wave_max(float x) {
  x = fmaxf(x, dpp<XOR1>(x)); x = fmaxf(x, dpp<XOR2>(x)); x = fmaxf(x, dpp<XOR7>(x)); x = fmaxf(x, dpp<XOR8>(x));
  auto s = __builtin_amdgcn_permlane16_swap(__float_as_uint(x), __float_as_uint(x), false, false);
  x = fmaxf(__uint_as_float(s[0]), __uint_as_float(s[1]));
  auto t = __builtin_amdgcn_permlane32_swap(__float_as_uint(x), __float_as_uint(x), false, false);
  return fmaxf(__uint_as_float(t[0]), __uint_as_float(t[1]));
}
__device__ __forceinline__ float wave_sum(float x) {
  x += dpp<XOR1>(x); x += dpp<XOR2>(x); x += dpp<XOR7>(x); x += dpp<XOR8>(x);
  auto s = __builtin_amdgcn_permlane16_swap(__float_as_uint(x), __float_as_uint(x), false, false);
  x = __uint_as_float(s[0]) + __uint_as_float(s[1]);
  auto t = __builtin_amdgcn_permlane32_swap(__float_as_uint(x), __float_as_uint(x), false, false);
  return __uint_as_float(t[0]) + __uint_as_float(t[1]);
}

constexpr int L_E = 0;
constexpr int L_PART = L_E + NW * 2048;
constexpr int L_P = L_PART + NW * 256;
constexpr int L_O = L_P + NW * 128;
constexpr int L_Q = L_O + NW * 512;
constexpr int L_TOTAL = L_Q + NW * 256;

struct Ctx {
  unsigned kvoff, efoff, ewr, erd;
  LAS char* lds;
};

template <bool ISK>
__device__ __forceinline__ void slab(const u32x4 (&kb)[8], const u32x4 (&ef)[4], f32x4 (&acc)[4][8]) {
#pragma unroll
  for (int m = 0; m < 4; ++m) {
    u32x4 f0, f1;
#pragma unroll
    for (int p = 0; p < 4; ++p) {
      const unsigned a = kb[2 * p][m], b = kb[2 * p + 1][m];
      f0[p] = __builtin_amdgcn_perm(b, a, 0x05040100u);
      f1[p] = __builtin_amdgcn_perm(b, a, 0x07060302u);
    }
#pragma unroll
    for (int rt = 0; rt < 4; ++rt) {
      if (ISK) {
        acc[rt][2 * m] = mfma(f0, ef[rt], acc[rt][2 * m]);
        acc[rt][2 * m + 1] = mfma(f1, ef[rt], acc[rt][2 * m + 1]);
      } else {
        acc[rt][2 * m] = mfma(ef[rt], f0, acc[rt][2 * m]);
        acc[rt][2 * m + 1] = mfma(ef[rt], f1, acc[rt][2 * m + 1]);
      }
    }
  }
}

__device__ __forceinline__ void issue_kv(u32x4 (&kb)[8], rsrc_t r, unsigned voff, unsigned so) {
#pragma unroll
  for (int j = 0; j < 8; ++j) kb[j] = ldb<AUXKV>(r, voff + j * 256, so);
}
__device__ __forceinline__ void issue_ef(u32x4 (&er)[8], rsrc_t r, unsigned voff, unsigned so) {
#pragma unroll
  for (int j = 0; j < 8; ++j) er[j] = ldb<0>(r, voff, so + j * (8 * SKV * 2));
}

template <bool ISK>
__device__ __forceinline__ void block64(const Ctx& cx, u32x4 (&kA)[8], u32x4 (&kB)[8], u32x4 (&er)[8], f32x4 (&acc)[4][8],
                                        rsrc_t rkv_n, rsrc_t ref_n, unsigned blk_n) {
#pragma unroll
  for (int j = 0; j < 8; ++j) *(LAS u32x4*)(cx.lds + (cx.ewr ^ ((j & 1) << 6)) + j * 1024) = er[j];
  u32x4 ef[4];
#pragma unroll
  for (int rt = 0; rt < 4; ++rt) ef[rt] = *(LAS u32x4*)(cx.lds + cx.erd + rt * 2048);
  slab<ISK>(kA, ef, acc);
  __builtin_amdgcn_sched_barrier(0);
  issue_ef(er, ref_n, cx.efoff, blk_n * 128);
  issue_kv(kA, rkv_n, cx.kvoff, blk_n * 16384);
  __builtin_amdgcn_sched_barrier(0);
#pragma unroll
  for (int rt = 0; rt < 4; ++rt) ef[rt] = *(LAS u32x4*)(cx.lds + (cx.erd ^ 64) + rt * 2048);
  slab<ISK>(kB, ef, acc);
  __builtin_amdgcn_sched_barrier(0);
  issue_kv(kB, rkv_n, cx.kvoff, blk_n * 16384 + 8192);
  __builtin_amdgcn_sched_barrier(0);
}

__global__ void __launch_bounds__(NW * 64, 1)
linformer_kernel(const char* __restrict__ q_, const char* __restrict__ k_, const char* __restrict__ v_,
                 const char* __restrict__ e_, const char* __restrict__ f_, char* __restrict__ out_
                 ) {
  __shared__ __attribute__((aligned(16))) float lds_raw[L_TOTAL];
  LAS float* lds = (LAS float*)lds_raw;
  const int head = blockIdx.x;
  const int tid = threadIdx.x;
  const int lane = tid & 63;
  const int w = __builtin_amdgcn_readfirstlane(tid >> 6);
  const int c = lane & 15, g = lane >> 4;

  const char* kh = k_ + (size_t)head * (SKV * DH * 2);
  const char* vh = v_ + (size_t)head * (SKV * DH * 2);
  Ctx cx;
  cx.lds = (LAS char*)lds;
  cx.kvoff = g * 2048 + c * 16;
  cx.efoff = (lane >> 3) * (SKV * 2) + (lane & 7) * 16;
  cx.ewr = w * 8192 + (lane >> 3) * 128 + (((lane & 7) ^ (lane >> 4)) << 4);
  cx.erd = w * 8192 + c * 128 + ((g ^ (c >> 1)) << 4);

  u32x4 kA[8], kB[8], er[8];
  f32x4 acc[4][8];

  const u32x4 qraw = *(const GAS u32x4*)((const GAS char*)q_ + (size_t)head * (SQ * DH * 2) + lane * 16);
  {
    const rsrc_t rk = mkrsrc(kh, SKV * DH * 2), re = mkrsrc(e_, RR * SKV * 2);
    issue_ef(er, re, cx.efoff, w * 128);
    issue_kv(kA, rk, cx.kvoff, w * 16384);
    issue_kv(kB, rk, cx.kvoff, w * 16384 + 8192);
  }
  __builtin_amdgcn_sched_barrier(0);
  *(LAS u32x4*)(lds + L_Q + w * 256 + lane * 4) = qraw;
#pragma unroll
  for (int rt = 0; rt < 4; ++rt)
#pragma unroll
    for (int cc = 0; cc < 8; ++cc) acc[rt][cc] = f32x4{0.f, 0.f, 0.f, 0.f};
  __builtin_amdgcn_sched_barrier(0);

#pragma nounroll
  for (int i = 0; i < NIT; ++i) {
    const bool last = i == NIT - 1;
    const rsrc_t rn = mkrsrc(last ? vh : kh, SKV * DH * 2), en = mkrsrc(last ? f_ : e_, RR * SKV * 2);
    block64<true>(cx, kA, kB, er, acc, rn, en, last ? w : w + NW * (i + 1));
  }
  {
    f32x4 sc[4];
#pragma unroll
    for (int rt = 0; rt < 4; ++rt) sc[rt] = f32x4{0.f, 0.f, 0.f, 0.f};
#pragma unroll
    for (int i = 0; i < 4; ++i) {
      u32x4 qf = *(LAS u32x4*)(lds + L_Q + w * 256 + (c & 3) * 64 + g * 16 + i * 4);
      if (c >= SQ) qf = u32x4{0u, 0u, 0u, 0u};
#pragma unroll
      for (int rt = 0; rt < 4; ++rt) {
        u32x4 hi, lo;
#pragma unroll
        for (int p = 0; p < 4; ++p) {
          const float x0 = acc[rt][2 * p][i], x1 = acc[rt][2 * p + 1][i];
          hi[p] = cvtpk(x0, x1);
          lo[p] = cvtpk(x0 - bflo(hi[p]), x1 - bfhi(hi[p]));
        }
        sc[rt] = mfma(hi, qf, sc[rt]);
        sc[rt] = mfma(lo, qf, sc[rt]);
      }
    }
    if (c < SQ) {
#pragma unroll
      for (int rt = 0; rt < 4; ++rt) *(LAS f32x4*)(lds + L_PART + w * 256 + c * 64 + rt * 16 + g * 4) = sc[rt];
    }
#pragma unroll
    for (int rt = 0; rt < 4; ++rt)
#pragma unroll
      for (int cc = 0; cc < 8; ++cc) acc[rt][cc] = f32x4{0.f, 0.f, 0.f, 0.f};
    __builtin_amdgcn_sched_barrier(0);
  }

  u32x4 pfrag[2] = {u32x4{0u, 0u, 0u, 0u}, u32x4{0u, 0u, 0u, 0u}};
#pragma nounroll
  for (int i = 0; i < NIT; ++i) {
    const bool last = i == NIT - 1;
    if (last) {
      asm volatile("s_waitcnt lgkmcnt(0)" ::: "memory");
      __builtin_amdgcn_s_barrier();
      asm volatile("" ::: "memory");
      LAS unsigned short* pl = (LAS unsigned short*)(lds + L_P + w * 128);
#pragma unroll
      for (int qq = 0; qq < SQ; ++qq) {
        float s = 0.f;
#pragma unroll
        for (int ww = 0; ww < NW; ++ww) s += lds[L_PART + ww * 256 + qq * 64 + lane];
        s *= 0.08838834764831845f;
        const float mx = wave_max(s);
        const float ex = __expf(s - mx);
        const float sum = wave_sum(ex);
        const float pv = ex / sum;
        pl[qq * 64 + lane] = (unsigned short)(cvtpk(pv, 0.f) & 0xffffu);
      }
#pragma unroll
      for (int s = 0; s < 2; ++s) {
        const u32x2 a = *(LAS u32x2*)(pl + (c & 3) * 64 + 32 * s + 4 * g);
        const u32x2 b = *(LAS u32x2*)(pl + (c & 3) * 64 + 32 * s + 16 + 4 * g);
        pfrag[s] = c < SQ ? u32x4{a.x, a.y, b.x, b.y} : u32x4{0u, 0u, 0u, 0u};
      }
      __builtin_amdgcn_sched_barrier(0);
    }
    const rsrc_t rn = mkrsrc(vh, last ? 0 : SKV * DH * 2), en = mkrsrc(f_, last ? 0 : RR * SKV * 2);
    block64<false>(cx, kA, kB, er, acc, rn, en, last ? w : w + NW * (i + 1));
  }

  f32x4 oacc[8];
#pragma unroll
  for (int cc = 0; cc < 8; ++cc) {
    f32x4 o = {0.f, 0.f, 0.f, 0.f};
#pragma unroll
    for (int s = 0; s < 2; ++s) {
      u32x4 b;
      b[0] = cvtpk(acc[2 * s][cc][0], acc[2 * s][cc][1]);
      b[1] = cvtpk(acc[2 * s][cc][2], acc[2 * s][cc][3]);
      b[2] = cvtpk(acc[2 * s + 1][cc][0], acc[2 * s + 1][cc][1]);
      b[3] = cvtpk(acc[2 * s + 1][cc][2], acc[2 * s + 1][cc][3]);
      o = mfma(pfrag[s], b, o);
    }
    oacc[cc] = o;
  }
  if (g == 0) {
#pragma unroll
    for (int qq = 0; qq < SQ; ++qq) {
      *(LAS f32x4*)(lds + L_O + w * 512 + qq * 128 + c * 8) = f32x4{oacc[0][qq], oacc[1][qq], oacc[2][qq], oacc[3][qq]};
      *(LAS f32x4*)(lds + L_O + w * 512 + qq * 128 + c * 8 + 4) = f32x4{oacc[4][qq], oacc[5][qq], oacc[6][qq], oacc[7][qq]};
    }
  }
  asm volatile("s_waitcnt lgkmcnt(0)" ::: "memory");
  __builtin_amdgcn_s_barrier();
  asm volatile("" ::: "memory");
  {
    float s0 = 0.f, s1 = 0.f;
#pragma unroll
    for (int ww = 0; ww < NW; ++ww) {
      const f32x2 x = *(LAS f32x2*)(lds + L_O + ww * 512 + tid * 2);
      s0 += x.x; s1 += x.y;
    }
    *(GAS unsigned*)((GAS char*)out_ + (size_t)head * (SQ * DH * 2) + tid * 4) = cvtpk(s0, s1);
  }
}

}

extern "C" void kernel_launch(void* const* d_in, const int* in_sizes, int n_in, void* d_out, int out_size,
                              void* d_ws, size_t ws_size, hipStream_t stream) {
  (void)in_sizes; (void)n_in; (void)out_size; (void)d_ws; (void)ws_size;
  lin::linformer_kernel<<<lin::NHEAD, lin::NW * 64, 0, stream>>>((const char*)d_in[0], (const char*)d_in[1], (const char*)d_in[2],
                                                           (const char*)d_in[3], (const char*)d_in[4], (char*)d_out
      );
}
```

```cpp
#define AUXKV 19
#define TKMODE 1
#define TKWG 0
#include <hip/hip_runtime.h>
#include <hip/hip_bf16.h>
#include <stdint.h>

#define KLO 1
#define PRETOUCH 0
#define DELAY 0
#define STAMP 0

namespace lin {

constexpr int NW = 4;
constexpr int SQ = 4, SKV = 2048, DH = 128, RR = 64;
constexpr int NHEAD = 256;
constexpr int NIT = SKV / 64 / NW;

#define GAS __attribute__((address_space(1)))
#define LAS __attribute__((address_space(3)))
typedef unsigned u32x4 __attribute__((ext_vector_type(4)));
typedef unsigned u32x2 __attribute__((ext_vector_type(2)));
typedef float f32x4 __attribute__((ext_vector_type(4)));
typedef __bf16 bf16x8 __attribute__((ext_vector_type(8)));
typedef __bf16 bf16x2 __attribute__((ext_vector_type(2)));
typedef float f32x2 __attribute__((ext_vector_type(2)));
typedef __amdgpu_buffer_rsrc_t rsrc_t;

template <int AUX> __device__ __forceinline__ u32x4 ldb(rsrc_t r, unsigned voff, unsigned soff) {
  return __builtin_amdgcn_raw_buffer_load_b128(r, voff, soff, AUX);
}
__device__ __forceinline__ rsrc_t mkrsrc(const char* p, unsigned bytes) {
  return __builtin_amdgcn_make_buffer_rsrc((void*)p, 0, bytes, 0x00020000);
}
__device__ __forceinline__ f32x4 mfma(u32x4 a, u32x4 b, f32x4 c) {
  return __builtin_amdgcn_mfma_f32_16x16x32_bf16(__builtin_bit_cast(bf16x8, a), __builtin_bit_cast(bf16x8, b), c, 0, 0, 0);
}
__device__ __forceinline__ unsigned cvtpk(float lo, float hi) {
  f32x2 v = {lo, hi};
  bf16x2 r = __builtin_convertvector(v, bf16x2);
  return __builtin_bit_cast(unsigned, r);
}
__device__ __forceinline__ float bflo(unsigned u) { return __builtin_bit_cast(float, u << 16); }
__device__ __forceinline__ float bfhi(unsigned u) { return __builtin_bit_cast(float, u & 0xffff0000u); }

template <int CTRL> __device__ __forceinline__ float dpp(float x) {
  return __builtin_bit_cast(float, __builtin_amdgcn_mov_dpp(__builtin_bit_cast(int, x), CTRL, 0xf, 0xf, true));
}
constexpr int XOR1 = 0xB1, XOR2 = 0x4E, XOR7 = 0x141, XOR8 = 0x128;
__device__ __forceinline__ float wave_max(float x) {
  x = fmaxf(x, dpp<XOR1>(x)); x = fmaxf(x, dpp<XOR2>(x)); x = fmaxf(x, dpp<XOR7>(x)); x = fmaxf(x, dpp<XOR8>(x));
  auto s = __builtin_amdgcn_permlane16_swap(__float_as_uint(x), __float_as_uint(x), false, false);
  x = fmaxf(__uint_as_float(s[0]), __uint_as_float(s[1]));
  auto t = __builtin_amdgcn_permlane32_swap(__float_as_uint(x), __float_as_uint(x), false, false);
  return fmaxf(__uint_as_float(t[0]), __uint_as_float(t[1]));
}
__device__ __forceinline__ float wave_sum(float x) {
  x += dpp<XOR1>(x); x += dpp<XOR2>(x); x += dpp<XOR7>(x); x += dpp<XOR8>(x);
  auto s = __builtin_amdgcn_permlane16_swap(__float_as_uint(x), __float_as_uint(x), false, false);
  x = __uint_as_float(s[0]) + __uint_as_float(s[1]);
  auto t = __builtin_amdgcn_permlane32_swap(__float_as_uint(x), __float_as_uint(x), false, false);
  return __uint_as_float(t[0]) + __uint_as_float(t[1]);
}

constexpr int L_E = 0;
constexpr int L_PART = L_E + NW * 2048;
constexpr int L_P = L_PART + NW * 256;
constexpr int L_O = L_P + NW * 128;
constexpr int L_Q = L_O + NW * 512;
constexpr int L_TOTAL = L_Q + NW * 256;

struct Ctx {
  unsigned kvoff, efoff, ewr, erd;
  LAS char* lds;
};

template <bool ISK>
__device__ __forceinline__ void slab(const u32x4 (&kb)[8], const u32x4 (&ef)[4], f32x4 (&acc)[4][8]) {
#pragma unroll
  for (int m = 0; m < 4; ++m) {
    u32x4 f0, f1;
#pragma unroll
    for (int p = 0; p < 4; ++p) {
      const unsigned a = kb[2 * p][m], b = kb[2 * p + 1][m];
      f0[p] = __builtin_amdgcn_perm(b, a, 0x05040100u);
      f1[p] = __builtin_amdgcn_perm(b, a, 0x07060302u);
    }
#pragma unroll
    for (int rt = 0; rt < 4; ++rt) {
      if (ISK) {
        acc[rt][2 * m] = mfma(f0, ef[rt], acc[rt][2 * m]);
        acc[rt][2 * m + 1] = mfma(f1, ef[rt], acc[rt][2 * m + 1]);
      } else {
        acc[rt][2 * m] = mfma(ef[rt], f0, acc[rt][2 * m]);
        acc[rt][2 * m + 1] = mfma(ef[rt], f1, acc[rt][2 * m + 1]);
      }
    }
  }
}

__device__ __forceinline__ void issue_kv(u32x4 (&kb)[8], rsrc_t r, unsigned voff, unsigned so) {
#pragma unroll
  for (int j = 0; j < 8; ++j) kb[j] = ldb<AUXKV>(r, voff + j * 256, so);
}
__device__ __forceinline__ void issue_ef(u32x4 (&er)[8], rsrc_t r, unsigned voff, unsigned so) {
#pragma unroll
  for (int j = 0; j < 8; ++j) er[j] = ldb<17>(r, voff, so + j * (8 * SKV * 2));
}

template <bool ISK>
__device__ __forceinline__ void block64(const Ctx& cx, u32x4 (&kA)[8], u32x4 (&kB)[8], u32x4 (&er)[8], f32x4 (&acc)[4][8],
                                        rsrc_t rkv_n, rsrc_t ref_n, unsigned blk_n) {
#pragma unroll
  for (int j = 0; j < 8; ++j) *(LAS u32x4*)(cx.lds + (cx.ewr ^ ((j & 1) << 6)) + j * 1024) = er[j];
  u32x4 ef[4];
#pragma unroll
  for (int rt = 0; rt < 4; ++rt) ef[rt] = *(LAS u32x4*)(cx.lds + cx.erd + rt * 2048);
  slab<ISK>(kA, ef, acc);
  __builtin_amdgcn_sched_barrier(0);
  issue_ef(er, ref_n, cx.efoff, blk_n * 128);
  issue_kv(kA, rkv_n, cx.kvoff, blk_n * 16384);
  __builtin_amdgcn_sched_barrier(0);
#pragma unroll
  for (int rt = 0; rt < 4; ++rt) ef[rt] = *(LAS u32x4*)(cx.lds + (cx.erd ^ 64) + rt * 2048);
  slab<ISK>(kB, ef, acc);
  __builtin_amdgcn_sched_barrier(0);
  issue_kv(kB, rkv_n, cx.kvoff, blk_n * 16384 + 8192);
  __builtin_amdgcn_sched_barrier(0);
}

__global__ void __launch_bounds__(NW * 64, 1)
linformer_kernel(const char* __restrict__ q_, const char* __restrict__ k_, const char* __restrict__ v_,
                 const char* __restrict__ e_, const char* __restrict__ f_, char* __restrict__ out_
                 ) {
  unsigned long long tk0 = 0;
  asm volatile("s_memrealtime %0\n\ts_waitcnt lgkmcnt(0)" : "=s"(tk0) :: "memory");
  __shared__ __attribute__((aligned(16))) float lds_raw[L_TOTAL];
  LAS float* lds = (LAS float*)lds_raw;
  const int head = blockIdx.x;
  const int tid = threadIdx.x;
  const int lane = tid & 63;
  const int w = __builtin_amdgcn_readfirstlane(tid >> 6);
  const int c = lane & 15, g = lane >> 4;

  const char* kh = k_ + (size_t)head * (SKV * DH * 2);
  const char* vh = v_ + (size_t)head * (SKV * DH * 2);
  Ctx cx;
  cx.lds = (LAS char*)lds;
  cx.kvoff = g * 2048 + c * 16;
  cx.efoff = (lane >> 3) * (SKV * 2) + (lane & 7) * 16;
  cx.ewr = w * 8192 + (lane >> 3) * 128 + (((lane & 7) ^ (lane >> 4)) << 4);
  cx.erd = w * 8192 + c * 128 + ((g ^ (c >> 1)) << 4);

  u32x4 kA[8], kB[8], er[8];
  f32x4 acc[4][8];

  const u32x4 qraw = *(const GAS u32x4*)((const GAS char*)q_ + (size_t)head * (SQ * DH * 2) + lane * 16);
  {
    const rsrc_t rk = mkrsrc(kh, SKV * DH * 2), re = mkrsrc(e_, RR * SKV * 2);
    issue_ef(er, re, cx.efoff, w * 128);
    issue_kv(kA, rk, cx.kvoff, w * 16384);
    issue_kv(kB, rk, cx.kvoff, w * 16384 + 8192);
  }
  __builtin_amdgcn_sched_barrier(0);
  *(LAS u32x4*)(lds + L_Q + w * 256 + lane * 4) = qraw;
#pragma unroll
  for (int rt = 0; rt < 4; ++rt)
#pragma unroll
    for (int cc = 0; cc < 8; ++cc) acc[rt][cc] = f32x4{0.f, 0.f, 0.f, 0.f};
  __builtin_amdgcn_sched_barrier(0);

#pragma nounroll
  for (int i = 0; i < NIT; ++i) {
    const bool last = i == NIT - 1;
    const rsrc_t rn = mkrsrc(last ? vh : kh, SKV * DH * 2), en = mkrsrc(last ? f_ : e_, RR * SKV * 2);
    block64<true>(cx, kA, kB, er, acc, rn, en, last ? w : w + NW * (i + 1));
  }
  {
    f32x4 sc[4];
#pragma unroll
    for (int rt = 0; rt < 4; ++rt) sc[rt] = f32x4{0.f, 0.f, 0.f, 0.f};
#pragma unroll
    for (int i = 0; i < 4; ++i) {
      u32x4 qf = *(LAS u32x4*)(lds + L_Q + w * 256 + (c & 3) * 64 + g * 16 + i * 4);
      if (c >= SQ) qf = u32x4{0u, 0u, 0u, 0u};
#pragma unroll
      for (int rt = 0; rt < 4; ++rt) {
        u32x4 hi, lo;
#pragma unroll
        for (int p = 0; p < 4; ++p) {
          const float x0 = acc[rt][2 * p][i], x1 = acc[rt][2 * p + 1][i];
          hi[p] = cvtpk(x0, x1);
          lo[p] = cvtpk(x0 - bflo(hi[p]), x1 - bfhi(hi[p]));
        }
        sc[rt] = mfma(hi, qf, sc[rt]);
        sc[rt] = mfma(lo, qf, sc[rt]);
      }
    }
    if (c < SQ) {
#pragma unroll
      for (int rt = 0; rt < 4; ++rt) *(LAS f32x4*)(lds + L_PART + w * 256 + c * 64 + rt * 16 + g * 4) = sc[rt];
    }
#pragma unroll
    for (int rt = 0; rt < 4; ++rt)
#pragma unroll
      for (int cc = 0; cc < 8; ++cc) acc[rt][cc] = f32x4{0.f, 0.f, 0.f, 0.f};
    __builtin_amdgcn_sched_barrier(0);
  }

  u32x4 pfrag[2] = {u32x4{0u, 0u, 0u, 0u}, u32x4{0u, 0u, 0u, 0u}};
#pragma nounroll
  for (int i = 0; i < NIT; ++i) {
    const bool last = i == NIT - 1;
    if (last) {
      asm volatile("s_waitcnt lgkmcnt(0)" ::: "memory");
      __builtin_amdgcn_s_barrier();
      asm volatile("" ::: "memory");
      LAS unsigned short* pl = (LAS unsigned short*)(lds + L_P + w * 128);
#pragma unroll
      for (int qq = 0; qq < SQ; ++qq) {
        float s = 0.f;
#pragma unroll
        for (int ww = 0; ww < NW; ++ww) s += lds[L_PART + ww * 256 + qq * 64 + lane];
        s *= 0.08838834764831845f;
        const float mx = wave_max(s);
        const float ex = __expf(s - mx);
        const float sum = wave_sum(ex);
        const float pv = ex / sum;
        pl[qq * 64 + lane] = (unsigned short)(cvtpk(pv, 0.f) & 0xffffu);
      }
#pragma unroll
      for (int s = 0; s < 2; ++s) {
        const u32x2 a = *(LAS u32x2*)(pl + (c & 3) * 64 + 32 * s + 4 * g);
        const u32x2 b = *(LAS u32x2*)(pl + (c & 3) * 64 + 32 * s + 16 + 4 * g);
        pfrag[s] = c < SQ ? u32x4{a.x, a.y, b.x, b.y} : u32x4{0u, 0u, 0u, 0u};
      }
      __builtin_amdgcn_sched_barrier(0);
    }
    const rsrc_t rn = mkrsrc(vh, last ? 0 : SKV * DH * 2), en = mkrsrc(f_, last ? 0 : RR * SKV * 2);
    block64<false>(cx, kA, kB, er, acc, rn, en, last ? w : w + NW * (i + 1));
  }

  f32x4 oacc[8];
#pragma unroll
  for (int cc = 0; cc < 8; ++cc) {
    f32x4 o = {0.f, 0.f, 0.f, 0.f};
#pragma unroll
    for (int s = 0; s < 2; ++s) {
      u32x4 b;
      b[0] = cvtpk(acc[2 * s][cc][0], acc[2 * s][cc][1]);
      b[1] = cvtpk(acc[2 * s][cc][2], acc[2 * s][cc][3]);
      b[2] = cvtpk(acc[2 * s + 1][cc][0], acc[2 * s + 1][cc][1]);
      b[3] = cvtpk(acc[2 * s + 1][cc][2], acc[2 * s + 1][cc][3]);
      o = mfma(pfrag[s], b, o);
    }
    oacc[cc] = o;
  }
  if (g == 0) {
#pragma unroll
    for (int qq = 0; qq < SQ; ++qq) {
      *(LAS f32x4*)(lds + L_O + w * 512 + qq * 128 + c * 8) = f32x4{oacc[0][qq], oacc[1][qq], oacc[2][qq], oacc[3][qq]};
      *(LAS f32x4*)(lds + L_O + w * 512 + qq * 128 + c * 8 + 4) = f32x4{oacc[4][qq], oacc[5][qq], oacc[6][qq], oacc[7][qq]};
    }
  }
  asm volatile("s_waitcnt lgkmcnt(0)" ::: "memory");
  __builtin_amdgcn_s_barrier();
  asm volatile("" ::: "memory");
  {
    float s0 = 0.f, s1 = 0.f;
#pragma unroll
    for (int ww = 0; ww < NW; ++ww) {
      const f32x2 x = *(LAS f32x2*)(lds + L_O + ww * 512 + tid * 2);
      s0 += x.x; s1 += x.y;
    }
    *(GAS unsigned*)((GAS char*)out_ + (size_t)head * (SQ * DH * 2) + tid * 4) = cvtpk(s0, s1);
  }
  if (head == TKWG) { while (__builtin_amdgcn_s_memrealtime() - tk0 < 10000) __builtin_amdgcn_s_sleep(2); }
}

}

extern "C" void kernel_launch(void* const* d_in, const int* in_sizes, int n_in, void* d_out, int out_size,
                              void* d_ws, size_t ws_size, hipStream_t stream) {
  (void)in_sizes; (void)n_in; (void)out_size; (void)d_ws; (void)ws_size;
  lin::linformer_kernel<<<lin::NHEAD, lin::NW * 64, 0, stream>>>((const char*)d_in[0], (const char*)d_in[1], (const char*)d_in[2],
                                                           (const char*)d_in[3], (const char*)d_in[4], (char*)d_out
      );
}
```

```cpp
#include <hip/hip_runtime.h>
#include <hip/hip_bf16.h>
#include <stdint.h>

#define ROT 1
#define XMAP 1
#define STAMP 0

namespace lin {

constexpr int NW = 4;
constexpr int SQ = 4, SKV = 2048, DH = 128, RR = 64;
constexpr int NHEAD = 256;
constexpr int NB = SKV / 64 / NW;

#define GAS __attribute__((address_space(1)))
#define LAS __attribute__((address_space(3)))
typedef unsigned u32x4 __attribute__((ext_vector_type(4)));
typedef unsigned u32x8 __attribute__((ext_vector_type(8)));
typedef unsigned u32x32 __attribute__((ext_vector_type(32)));
typedef float f32x4 __attribute__((ext_vector_type(4)));
typedef float f32x16 __attribute__((ext_vector_type(16)));
typedef float f32x32 __attribute__((ext_vector_type(32)));
typedef __bf16 bf16x2 __attribute__((ext_vector_type(2)));
typedef float f32x2 __attribute__((ext_vector_type(2)));

__device__ __forceinline__ u32x4 mkdesc(const char* p, unsigned bytes) {
  const unsigned long long a = (unsigned long long)p;
  return u32x4{(unsigned)a, (unsigned)(a >> 32) & 0xffffu, bytes, 0x00020000u};
}
__device__ __forceinline__ unsigned cvtpk(float lo, float hi) {
  f32x2 v = {lo, hi};
  bf16x2 r = __builtin_convertvector(v, bf16x2);
  return __builtin_bit_cast(unsigned, r);
}
template <int CTRL> __device__ __forceinline__ float dpp(float x) {
  return __builtin_bit_cast(float, __builtin_amdgcn_mov_dpp(__builtin_bit_cast(int, x), CTRL, 0xf, 0xf, true));
}
constexpr int XOR1 = 0xB1, XOR2 = 0x4E, XOR7 = 0x141, XOR8 = 0x128, ROR4 = 0x124;
__device__ __forceinline__ float wave_max(float x) {
  x = fmaxf(x, dpp<XOR1>(x)); x = fmaxf(x, dpp<XOR2>(x)); x = fmaxf(x, dpp<XOR7>(x)); x = fmaxf(x, dpp<XOR8>(x));
  auto s = __builtin_amdgcn_permlane16_swap(__float_as_uint(x), __float_as_uint(x), false, false);
  x = fmaxf(__uint_as_float(s[0]), __uint_as_float(s[1]));
  auto t = __builtin_amdgcn_permlane32_swap(__float_as_uint(x), __float_as_uint(x), false, false);
  return fmaxf(__uint_as_float(t[0]), __uint_as_float(t[1]));
}
__device__ __forceinline__ float wave_sum(float x) {
  x += dpp<XOR1>(x); x += dpp<XOR2>(x); x += dpp<XOR7>(x); x += dpp<XOR8>(x);
  auto s = __builtin_amdgcn_permlane16_swap(__float_as_uint(x), __float_as_uint(x), false, false);
  x = __uint_as_float(s[0]) + __uint_as_float(s[1]);
  auto t = __builtin_amdgcn_permlane32_swap(__float_as_uint(x), __float_as_uint(x), false, false);
  return __uint_as_float(t[0]) + __uint_as_float(t[1]);
}

constexpr int L_E = 0;
constexpr int L_PART = L_E + NW * 2048;
constexpr int L_P = L_PART + NW * 256;
constexpr int L_O = L_P + NW * 128;
constexpr int L_TOTAL = L_O + NW * 512;

#define RING_OPS(M)                                                                                       \
  M "{v[128:159]}"(r0), M "{v[160:191]}"(r1), M "{v[192:223]}"(r2), M "{v[224:255]}"(r3),                   \
  M "{a[0:31]}"(e01), M "{a[32:63]}"(e23)
#define LOAD_OPS                                                                                          \
  [vo0] "v"(vo0), [vo1] "v"(vo1), [ef0] "v"(ef0), [ef1] "v"(ef1), [ef2] "v"(ef2), [ef3] "v"(ef3),           \
  [rkv] "s"(rkv), [re] "s"(re), [re4] "s"(re4), [so0] "s"(bA * 16384), [so1] "s"(bA * 16384 + 8192),       \
  [so2] "s"(bB * 16384), [so3] "s"(bB * 16384 + 8192), [se0] "s"(bA * 128 + 131072), [se1] "s"(bB * 128),  \
  [se2] "s"(bB * 128 + 131072), [se3] "s"(bC * 128)
#define T_CLOB                                                                                            \
  "v52", "v53", "v54", "v55", "v56", "v57", "v58", "v59", "v60", "v61", "v62", "v63",                      \
  "v96", "v97", "v98", "v99", "v100", "v101", "v102", "v103", "v104", "v105", "v106", "v107", "v108",      \
  "v109", "v110", "v111", "v112", "v113", "v114", "v115", "v116", "v117", "v118", "v119", "v120", "v121",  \
  "v122", "v123", "v124", "v125", "v126", "v127", "a64", "a65", "a66", "a67", "a68", "a69", "a70", "a71",  \
  "a72", "a73", "a74", "a75", "a76", "a77", "a78", "a79"

__global__ void __launch_bounds__(NW * 64, 1)
linformer_kernel(const char* __restrict__ q_, const char* __restrict__ k_, const char* __restrict__ v_,
                 const char* __restrict__ e_, const char* __restrict__ f_, char* __restrict__ out_
                 ) {
#define TL(idx)
  __shared__ __attribute__((aligned(16))) float lds_raw[L_TOTAL];
  LAS float* lds = (LAS float*)lds_raw;
  const unsigned lbase = (unsigned)(unsigned long long)lds;
  const int bid = blockIdx.x;
  const int head = XMAP ? (bid & 7) * 32 + (bid >> 3) : bid;
  const int tid = threadIdx.x;
  const int lane = tid & 63;
  const int w = __builtin_amdgcn_readfirstlane(tid >> 6);
  const int c = lane & 15, g = lane >> 4;
  const int rot = ROT ? (bid >> 3) & (NB - 1) : 0;
#define BLK(n) (w + NW * (((n) + rot) & (NB - 1)))

  const char* kh = k_ + (size_t)head * (SKV * DH * 2);
  const char* vh = v_ + (size_t)head * (SKV * DH * 2);
  const unsigned voK = (c >> 2) * 2048 + g * 64 + (c & 3) * 16;
  const unsigned voV = g * 1024 + c * 16;
  const unsigned ef0 = (lane >> 3) * (SKV * 2) + (lane & 7) * 16;
  const unsigned ef1 = ef0 + 32768, ef2 = ef0 + 65536, ef3 = ef0 + 98304;
  const unsigned tile = lbase + w * 8192;
  const unsigned ewr0 = tile + (lane >> 3) * 128 + (((lane & 7) ^ ((lane >> 3) & 2)) << 4), ewr1 = ewr0 ^ 64;

  u32x32 r0, r1, r2, r3, e01, e23;
  u32x4 qb;
  f32x16 sacc;

  {
    const u32x4 rkv = mkdesc(kh, SKV * DH * 2), re = mkdesc(e_, RR * SKV * 2), re4 = re;
    const int bA = BLK(0), bB = BLK(1), bC = BLK(2);
    const unsigned vo0 = voK, vo1 = voK + 1024;
    asm volatile(
    "global_load_dwordx4 v[48:51], %[qoff], %[qb]\n\t"
    "buffer_load_dwordx4 a[80:83], %[ef0], %[re], %[seP] offen\n\t"
    "buffer_load_dwordx4 a[84:87], %[ef1], %[re], %[seP] offen\n\t"
    "buffer_load_dwordx4 a[88:91], %[ef2], %[re], %[seP] offen\n\t"
    "buffer_load_dwordx4 a[92:95], %[ef3], %[re], %[seP] offen\n\t"
    "buffer_load_dwordx4 v[128:131], %[vo0], %[rkv], %[so0] offen nt\n\t"
    "buffer_load_dwordx4 v[132:135], %[vo0], %[rkv], %[so0] offen offset:256 nt\n\t"
    "buffer_load_dwordx4 a[0:3], %[ef0], %[re], %[se0] offen\n\t"
    "buffer_load_dwordx4 v[136:139], %[vo0], %[rkv], %[so0] offen offset:512 nt\n\t"
    "buffer_load_dwordx4 v[140:143], %[vo0], %[rkv], %[so0] offen offset:768 nt\n\t"
    "buffer_load_dwordx4 a[4:7], %[ef1], %[re], %[se0] offen\n\t"
    "buffer_load_dwordx4 v[144:147], %[vo1], %[rkv], %[so0] offen nt\n\t"
    "buffer_load_dwordx4 v[148:151], %[vo1], %[rkv], %[so0] offen offset:256 nt\n\t"
    "buffer_load_dwordx4 a[8:11], %[ef2], %[re], %[se0] offen\n\t"
    "buffer_load_dwordx4 v[152:155], %[vo1], %[rkv], %[so0] offen offset:512 nt\n\t"
    "buffer_load_dwordx4 v[156:159], %[vo1], %[rkv], %[so0] offen offset:768 nt\n\t"
    "buffer_load_dwordx4 a[12:15], %[ef3], %[re], %[se0] offen\n\t"
    "buffer_load_dwordx4 v[160:163], %[vo0], %[rkv], %[so1] offen nt\n\t"
    "buffer_load_dwordx4 v[164:167], %[vo0], %[rkv], %[so1] offen offset:256 nt\n\t"
    "buffer_load_dwordx4 a[16:19], %[ef0], %[re], %[se1] offen\n\t"
    "buffer_load_dwordx4 v[168:171], %[vo0], %[rkv], %[so1] offen offset:512 nt\n\t"
    "buffer_load_dwordx4 v[172:175], %[vo0], %[rkv], %[so1] offen offset:768 nt\n\t"
    "buffer_load_dwordx4 a[20:23], %[ef1], %[re], %[se1] offen\n\t"
    "buffer_load_dwordx4 v[176:179], %[vo1], %[rkv], %[so1] offen nt\n\t"
    "buffer_load_dwordx4 v[180:183], %[vo1], %[rkv], %[so1] offen offset:256 nt\n\t"
    "buffer_load_dwordx4 a[24:27], %[ef2], %[re], %[se1] offen\n\t"
    "buffer_load_dwordx4 v[184:187], %[vo1], %[rkv], %[so1] offen offset:512 nt\n\t"
    "buffer_load_dwordx4 v[188:191], %[vo1], %[rkv], %[so1] offen offset:768 nt\n\t"
    "buffer_load_dwordx4 a[28:31], %[ef3], %[re], %[se1] offen\n\t"
    "buffer_load_dwordx4 v[192:195], %[vo0], %[rkv], %[so2] offen nt\n\t"
    "buffer_load_dwordx4 v[196:199], %[vo0], %[rkv], %[so2] offen offset:256 nt\n\t"
    "buffer_load_dwordx4 a[32:35], %[ef0], %[re], %[se2] offen\n\t"
    "buffer_load_dwordx4 v[200:203], %[vo0], %[rkv], %[so2] offen offset:512 nt\n\t"
    "buffer_load_dwordx4 v[204:207], %[vo0], %[rkv], %[so2] offen offset:768 nt\n\t"
    "buffer_load_dwordx4 a[36:39], %[ef1], %[re], %[se2] offen\n\t"
    "buffer_load_dwordx4 v[208:211], %[vo1], %[rkv], %[so2] offen nt\n\t"
    "buffer_load_dwordx4 v[212:215], %[vo1], %[rkv], %[so2] offen offset:256 nt\n\t"
    "buffer_load_dwordx4 a[40:43], %[ef2], %[re], %[se2] offen\n\t"
    "buffer_load_dwordx4 v[216:219], %[vo1], %[rkv], %[so2] offen offset:512 nt\n\t"
    "buffer_load_dwordx4 v[220:223], %[vo1], %[rkv], %[so2] offen offset:768 nt\n\t"
    "buffer_load_dwordx4 a[44:47], %[ef3], %[re], %[se2] offen\n\t"
    "buffer_load_dwordx4 v[224:227], %[vo0], %[rkv], %[so3] offen nt\n\t"
    "buffer_load_dwordx4 v[228:231], %[vo0], %[rkv], %[so3] offen offset:256 nt\n\t"
    "buffer_load_dwordx4 a[48:51], %[ef0], %[re4], %[se3] offen\n\t"
    "buffer_load_dwordx4 v[232:235], %[vo0], %[rkv], %[so3] offen offset:512 nt\n\t"
    "buffer_load_dwordx4 v[236:239], %[vo0], %[rkv], %[so3] offen offset:768 nt\n\t"
    "buffer_load_dwordx4 a[52:55], %[ef1], %[re4], %[se3] offen\n\t"
    "buffer_load_dwordx4 v[240:243], %[vo1], %[rkv], %[so3] offen nt\n\t"
    "buffer_load_dwordx4 v[244:247], %[vo1], %[rkv], %[so3] offen offset:256 nt\n\t"
    "buffer_load_dwordx4 a[56:59], %[ef2], %[re4], %[se3] offen\n\t"
    "buffer_load_dwordx4 v[248:251], %[vo1], %[rkv], %[so3] offen offset:512 nt\n\t"
    "buffer_load_dwordx4 v[252:255], %[vo1], %[rkv], %[so3] offen offset:768 nt\n\t"
    "buffer_load_dwordx4 a[60:63], %[ef3], %[re4], %[se3] offen\n\t"
    "v_mov_b32 v64, 0\n\t"
    "v_mov_b32 v65, 0\n\t"
    "v_mov_b32 v66, 0\n\t"
    "v_mov_b32 v67, 0\n\t"
    "v_mov_b32 v68, 0\n\t"
    "v_mov_b32 v69, 0\n\t"
    "v_mov_b32 v70, 0\n\t"
    "v_mov_b32 v71, 0\n\t"
    "v_mov_b32 v72, 0\n\t"
    "v_mov_b32 v73, 0\n\t"
    "v_mov_b32 v74, 0\n\t"
    "v_mov_b32 v75, 0\n\t"
    "v_mov_b32 v76, 0\n\t"
    "v_mov_b32 v77, 0\n\t"
    "v_mov_b32 v78, 0\n\t"
    "v_mov_b32 v79, 0\n\t"
    "s_waitcnt vmcnt(48)\n\t"
    "ds_write_b128 %[ewr0], a[80:83]\n\t"
    "ds_write_b128 %[ewr1], a[84:87] offset:1024\n\t"
    "ds_write_b128 %[ewr0], a[88:91] offset:2048\n\t"
    "ds_write_b128 %[ewr1], a[92:95] offset:3072\n\t"
        : RING_OPS("=&"), "=&{v[48:51]}"(qb), "=&{v[64:79]}"(sacc)
        : LOAD_OPS, [ewr0] "v"(ewr0), [ewr1] "v"(ewr1), [seP] "s"(bA * 128),
          [qb] "s"(q_ + (size_t)head * (SQ * DH * 2)), [qoff] "v"((c & 3) * 256 + g * 64 + (c >> 2) * 16)
        : "memory", "a80", "a81", "a82", "a83", "a84", "a85", "a86", "a87", "a88", "a89", "a90", "a91", "a92", "a93",
          "a94", "a95");
  }
  TL(0);

  {
    const unsigned erd0 = tile + c * 128 + ((g ^ (c & 2) ^ ((c & 8) >> 1)) << 4), erd1 = erd0 ^ 64;
#pragma nounroll
    for (int i = 0; i < NB; i += 2) {
      const bool nk = i + 2 < NB;
      const u32x4 rkv = mkdesc(nk ? kh : vh, SKV * DH * 2);
      const u32x4 re = mkdesc(nk ? e_ : f_, RR * SKV * 2), re4 = mkdesc(i + 4 < NB ? e_ : f_, RR * SKV * 2);
      const int bA = BLK(i + 2), bB = BLK(i + 3), bC = BLK(i + 4);
      const unsigned vo0 = nk ? voK : voV, vo1 = nk ? voK + 1024 : voV + 4096;
      asm volatile(
    "s_waitcnt vmcnt(36)\n\t"
    "ds_write_b128 %[ewr0], a[0:3] offset:4096\n\t"
    "ds_write_b128 %[ewr1], a[4:7] offset:5120\n\t"
    "ds_write_b128 %[ewr0], a[8:11] offset:6144\n\t"
    "ds_write_b128 %[ewr1], a[12:15] offset:7168\n\t"
    "ds_read_b128 a[64:67], %[erd0]\n\t"
    "ds_read_b128 a[68:71], %[erd0] offset:2048\n\t"
    "ds_read_b128 a[72:75], %[erd0] offset:4096\n\t"
    "ds_read_b128 a[76:79], %[erd0] offset:6144\n\t"
    "v_mfma_f32_16x16x32_bf16 v[96:99], v[128:131], v[48:51], 0\n\t"
    "v_mfma_f32_16x16x32_bf16 v[100:103], v[132:135], v[48:51], 0\n\t"
    "v_mfma_f32_16x16x32_bf16 v[104:107], v[136:139], v[48:51], 0\n\t"
    "v_mfma_f32_16x16x32_bf16 v[108:111], v[140:143], v[48:51], 0\n\t"
    "v_mfma_f32_16x16x32_bf16 v[112:115], v[144:147], v[48:51], 0\n\t"
    "v_mfma_f32_16x16x32_bf16 v[116:119], v[148:151], v[48:51], 0\n\t"
    "v_mfma_f32_16x16x32_bf16 v[120:123], v[152:155], v[48:51], 0\n\t"
    "v_mfma_f32_16x16x32_bf16 v[124:127], v[156:159], v[48:51], 0\n\t"
    "s_nop 4\n\t"
    "s_mov_b32 exec_lo, 0x000f000f\n\t"
    "s_mov_b32 exec_hi, 0x000f000f\n\t"
    "v_cvt_pk_bf16_f32 v60, v96, v100\n\t"
    "v_cvt_pk_bf16_f32 v61, v104, v108\n\t"
    "v_cvt_pk_bf16_f32 v62, v112, v116\n\t"
    "v_cvt_pk_bf16_f32 v63, v120, v124\n\t"
    "s_mov_b32 exec_lo, 0x00f000f0\n\t"
    "s_mov_b32 exec_hi, 0x00f000f0\n\t"
    "v_cvt_pk_bf16_f32 v60, v97, v101\n\t"
    "v_cvt_pk_bf16_f32 v61, v105, v109\n\t"
    "v_cvt_pk_bf16_f32 v62, v113, v117\n\t"
    "v_cvt_pk_bf16_f32 v63, v121, v125\n\t"
    "s_mov_b32 exec_lo, 0x0f000f00\n\t"
    "s_mov_b32 exec_hi, 0x0f000f00\n\t"
    "v_cvt_pk_bf16_f32 v60, v98, v102\n\t"
    "v_cvt_pk_bf16_f32 v61, v106, v110\n\t"
    "v_cvt_pk_bf16_f32 v62, v114, v118\n\t"
    "v_cvt_pk_bf16_f32 v63, v122, v126\n\t"
    "s_mov_b32 exec_lo, 0xf000f000\n\t"
    "s_mov_b32 exec_hi, 0xf000f000\n\t"
    "v_cvt_pk_bf16_f32 v60, v99, v103\n\t"
    "v_cvt_pk_bf16_f32 v61, v107, v111\n\t"
    "v_cvt_pk_bf16_f32 v62, v115, v119\n\t"
    "v_cvt_pk_bf16_f32 v63, v123, v127\n\t"
    "s_mov_b64 exec, -1\n\t"
    "s_waitcnt lgkmcnt(0)\n\t"
    "s_nop 1\n\t"
    "v_mfma_f32_16x16x32_bf16 v[64:67], a[64:67], v[60:63], v[64:67]\n\t"
    "v_mfma_f32_16x16x32_bf16 v[68:71], a[68:71], v[60:63], v[68:71]\n\t"
    "v_mfma_f32_16x16x32_bf16 v[72:75], a[72:75], v[60:63], v[72:75]\n\t"
    "v_mfma_f32_16x16x32_bf16 v[76:79], a[76:79], v[60:63], v[76:79]\n\t"
    "buffer_load_dwordx4 v[128:131], %[vo0], %[rkv], %[so0] offen nt\n\t"
    "buffer_load_dwordx4 v[132:135], %[vo0], %[rkv], %[so0] offen offset:256 nt\n\t"
    "buffer_load_dwordx4 a[0:3], %[ef0], %[re], %[se0] offen\n\t"
    "buffer_load_dwordx4 v[136:139], %[vo0], %[rkv], %[so0] offen offset:512 nt\n\t"
    "buffer_load_dwordx4 v[140:143], %[vo0], %[rkv], %[so0] offen offset:768 nt\n\t"
    "buffer_load_dwordx4 a[4:7], %[ef1], %[re], %[se0] offen\n\t"
    "buffer_load_dwordx4 v[144:147], %[vo1], %[rkv], %[so0] offen nt\n\t"
    "buffer_load_dwordx4 v[148:151], %[vo1], %[rkv], %[so0] offen offset:256 nt\n\t"
    "buffer_load_dwordx4 a[8:11], %[ef2], %[re], %[se0] offen\n\t"
    "buffer_load_dwordx4 v[152:155], %[vo1], %[rkv], %[so0] offen offset:512 nt\n\t"
    "buffer_load_dwordx4 v[156:159], %[vo1], %[rkv], %[so0] offen offset:768 nt\n\t"
    "buffer_load_dwordx4 a[12:15], %[ef3], %[re], %[se0] offen\n\t"
    "s_waitcnt vmcnt(36)\n\t"
    "ds_read_b128 a[64:67], %[erd1]\n\t"
    "ds_read_b128 a[68:71], %[erd1] offset:2048\n\t"
    "ds_read_b128 a[72:75], %[erd1] offset:4096\n\t"
    "ds_read_b128 a[76:79], %[erd1] offset:6144\n\t"
    "ds_write_b128 %[ewr0], a[16:19]\n\t"
    "ds_write_b128 %[ewr1], a[20:23] offset:1024\n\t"
    "ds_write_b128 %[ewr0], a[24:27] offset:2048\n\t"
    "ds_write_b128 %[ewr1], a[28:31] offset:3072\n\t"
    "v_mfma_f32_16x16x32_bf16 v[96:99], v[160:163], v[48:51], 0\n\t"
    "v_mfma_f32_16x16x32_bf16 v[100:103], v[164:167], v[48:51], 0\n\t"
    "v_mfma_f32_16x16x32_bf16 v[104:107], v[168:171], v[48:51], 0\n\t"
    "v_mfma_f32_16x16x32_bf16 v[108:111], v[172:175], v[48:51], 0\n\t"
    "v_mfma_f32_16x16x32_bf16 v[112:115], v[176:179], v[48:51], 0\n\t"
    "v_mfma_f32_16x16x32_bf16 v[116:119], v[180:183], v[48:51], 0\n\t"
    "v_mfma_f32_16x16x32_bf16 v[120:123], v[184:187], v[48:51], 0\n\t"
    "v_mfma_f32_16x16x32_bf16 v[124:127], v[188:191], v[48:51], 0\n\t"
    "s_nop 4\n\t"
    "s_mov_b32 exec_lo, 0x000f000f\n\t"
    "s_mov_b32 exec_hi, 0x000f000f\n\t"
    "v_cvt_pk_bf16_f32 v60, v96, v100\n\t"
    "v_cvt_pk_bf16_f32 v61, v104, v108\n\t"
    "v_cvt_pk_bf16_f32 v62, v112, v116\n\t"
    "v_cvt_pk_bf16_f32 v63, v120, v124\n\t"
    "s_mov_b32 exec_lo, 0x00f000f0\n\t"
    "s_mov_b32 exec_hi, 0x00f000f0\n\t"
    "v_cvt_pk_bf16_f32 v60, v97, v101\n\t"
    "v_cvt_pk_bf16_f32 v61, v105, v109\n\t"
    "v_cvt_pk_bf16_f32 v62, v113, v117\n\t"
    "v_cvt_pk_bf16_f32 v63, v121, v125\n\t"
    "s_mov_b32 exec_lo, 0x0f000f00\n\t"
    "s_mov_b32 exec_hi, 0x0f000f00\n\t"
    "v_cvt_pk_bf16_f32 v60, v98, v102\n\t"
    "v_cvt_pk_bf16_f32 v61, v106, v110\n\t"
    "v_cvt_pk_bf16_f32 v62, v114, v118\n\t"
    "v_cvt_pk_bf16_f32 v63, v122, v126\n\t"
    "s_mov_b32 exec_lo, 0xf000f000\n\t"
    "s_mov_b32 exec_hi, 0xf000f000\n\t"
    "v_cvt_pk_bf16_f32 v60, v99, v103\n\t"
    "v_cvt_pk_bf16_f32 v61, v107, v111\n\t"
    "v_cvt_pk_bf16_f32 v62, v115, v119\n\t"
    "v_cvt_pk_bf16_f32 v63, v123, v127\n\t"
    "s_mov_b64 exec, -1\n\t"
    "s_waitcnt lgkmcnt(4)\n\t"
    "s_nop 1\n\t"
    "v_mfma_f32_16x16x32_bf16 v[64:67], a[64:67], v[60:63], v[64:67]\n\t"
    "v_mfma_f32_16x16x32_bf16 v[68:71], a[68:71], v[60:63], v[68:71]\n\t"
    "v_mfma_f32_16x16x32_bf16 v[72:75], a[72:75], v[60:63], v[72:75]\n\t"
    "v_mfma_f32_16x16x32_bf16 v[76:79], a[76:79], v[60:63], v[76:79]\n\t"
    "buffer_load_dwordx4 v[160:163], %[vo0], %[rkv], %[so1] offen nt\n\t"
    "buffer_load_dwordx4 v[164:167], %[vo0], %[rkv], %[so1] offen offset:256 nt\n\t"
    "buffer_load_dwordx4 a[16:19], %[ef0], %[re], %[se1] offen\n\t"
    "buffer_load_dwordx4 v[168:171], %[vo0], %[rkv], %[so1] offen offset:512 nt\n\t"
    "buffer_load_dwordx4 v[172:175], %[vo0], %[rkv], %[so1] offen offset:768 nt\n\t"
    "buffer_load_dwordx4 a[20:23], %[ef1], %[re], %[se1] offen\n\t"
    "buffer_load_dwordx4 v[176:179], %[vo1], %[rkv], %[so1] offen nt\n\t"
    "buffer_load_dwordx4 v[180:183], %[vo1], %[rkv], %[so1] offen offset:256 nt\n\t"
    "buffer_load_dwordx4 a[24:27], %[ef2], %[re], %[se1] offen\n\t"
    "buffer_load_dwordx4 v[184:187], %[vo1], %[rkv], %[so1] offen offset:512 nt\n\t"
    "buffer_load_dwordx4 v[188:191], %[vo1], %[rkv], %[so1] offen offset:768 nt\n\t"
    "buffer_load_dwordx4 a[28:31], %[ef3], %[re], %[se1] offen\n\t"
    "s_waitcnt vmcnt(36)\n\t"
    "ds_write_b128 %[ewr0], a[32:35] offset:4096\n\t"
    "ds_write_b128 %[ewr1], a[36:39] offset:5120\n\t"
    "ds_write_b128 %[ewr0], a[40:43] offset:6144\n\t"
    "ds_write_b128 %[ewr1], a[44:47] offset:7168\n\t"
    "ds_read_b128 a[64:67], %[erd0]\n\t"
    "ds_read_b128 a[68:71], %[erd0] offset:2048\n\t"
    "ds_read_b128 a[72:75], %[erd0] offset:4096\n\t"
    "ds_read_b128 a[76:79], %[erd0] offset:6144\n\t"
    "v_mfma_f32_16x16x32_bf16 v[96:99], v[192:195], v[48:51], 0\n\t"
    "v_mfma_f32_16x16x32_bf16 v[100:103], v[196:199], v[48:51], 0\n\t"
    "v_mfma_f32_16x16x32_bf16 v[104:107], v[200:203], v[48:51], 0\n\t"
    "v_mfma_f32_16x16x32_bf16 v[108:111], v[204:207], v[48:51], 0\n\t"
    "v_mfma_f32_16x16x32_bf16 v[112:115], v[208:211], v[48:51], 0\n\t"
    "v_mfma_f32_16x16x32_bf16 v[116:119], v[212:215], v[48:51], 0\n\t"
    "v_mfma_f32_16x16x32_bf16 v[120:123], v[216:219], v[48:51], 0\n\t"
    "v_mfma_f32_16x16x32_bf16 v[124:127], v[220:223], v[48:51], 0\n\t"
    "s_nop 4\n\t"
    "s_mov_b32 exec_lo, 0x000f000f\n\t"
    "s_mov_b32 exec_hi, 0x000f000f\n\t"
    "v_cvt_pk_bf16_f32 v60, v96, v100\n\t"
    "v_cvt_pk_bf16_f32 v61, v104, v108\n\t"
    "v_cvt_pk_bf16_f32 v62, v112, v116\n\t"
    "v_cvt_pk_bf16_f32 v63, v120, v124\n\t"
    "s_mov_b32 exec_lo, 0x00f000f0\n\t"
    "s_mov_b32 exec_hi, 0x00f000f0\n\t"
    "v_cvt_pk_bf16_f32 v60, v97, v101\n\t"
    "v_cvt_pk_bf16_f32 v61, v105, v109\n\t"
    "v_cvt_pk_bf16_f32 v62, v113, v117\n\t"
    "v_cvt_pk_bf16_f32 v63, v121, v125\n\t"
    "s_mov_b32 exec_lo, 0x0f000f00\n\t"
    "s_mov_b32 exec_hi, 0x0f000f00\n\t"
    "v_cvt_pk_bf16_f32 v60, v98, v102\n\t"
    "v_cvt_pk_bf16_f32 v61, v106, v110\n\t"
    "v_cvt_pk_bf16_f32 v62, v114, v118\n\t"
    "v_cvt_pk_bf16_f32 v63, v122, v126\n\t"
    "s_mov_b32 exec_lo, 0xf000f000\n\t"
    "s_mov_b32 exec_hi, 0xf000f000\n\t"
    "v_cvt_pk_bf16_f32 v60, v99, v103\n\t"
    "v_cvt_pk_bf16_f32 v61, v107, v111\n\t"
    "v_cvt_pk_bf16_f32 v62, v115, v119\n\t"
    "v_cvt_pk_bf16_f32 v63, v123, v127\n\t"
    "s_mov_b64 exec, -1\n\t"
    "s_waitcnt lgkmcnt(0)\n\t"
    "s_nop 1\n\t"
    "v_mfma_f32_16x16x32_bf16 v[64:67], a[64:67], v[60:63], v[64:67]\n\t"
    "v_mfma_f32_16x16x32_bf16 v[68:71], a[68:71], v[60:63], v[68:71]\n\t"
    "v_mfma_f32_16x16x32_bf16 v[72:75], a[72:75], v[60:63], v[72:75]\n\t"
    "v_mfma_f32_16x16x32_bf16 v[76:79], a[76:79], v[60:63], v[76:79]\n\t"
    "buffer_load_dwordx4 v[192:195], %[vo0], %[rkv], %[so2] offen nt\n\t"
    "buffer_load_dwordx4 v[196:199], %[vo0], %[rkv], %[so2] offen offset:256 nt\n\t"
    "buffer_load_dwordx4 a[32:35], %[ef0], %[re], %[se2] offen\n\t"
    "buffer_load_dwordx4 v[200:203], %[vo0], %[rkv], %[so2] offen offset:512 nt\n\t"
    "buffer_load_dwordx4 v[204:207], %[vo0], %[rkv], %[so2] offen offset:768 nt\n\t"
    "buffer_load_dwordx4 a[36:39], %[ef1], %[re], %[se2] offen\n\t"
    "buffer_load_dwordx4 v[208:211], %[vo1], %[rkv], %[so2] offen nt\n\t"
    "buffer_load_dwordx4 v[212:215], %[vo1], %[rkv], %[so2] offen offset:256 nt\n\t"
    "buffer_load_dwordx4 a[40:43], %[ef2], %[re], %[se2] offen\n\t"
    "buffer_load_dwordx4 v[216:219], %[vo1], %[rkv], %[so2] offen offset:512 nt\n\t"
    "buffer_load_dwordx4 v[220:223], %[vo1], %[rkv], %[so2] offen offset:768 nt\n\t"
    "buffer_load_dwordx4 a[44:47], %[ef3], %[re], %[se2] offen\n\t"
    "s_waitcnt vmcnt(36)\n\t"
    "ds_read_b128 a[64:67], %[erd1]\n\t"
    "ds_read_b128 a[68:71], %[erd1] offset:2048\n\t"
    "ds_read_b128 a[72:75], %[erd1] offset:4096\n\t"
    "ds_read_b128 a[76:79], %[erd1] offset:6144\n\t"
    "ds_write_b128 %[ewr0], a[48:51]\n\t"
    "ds_write_b128 %[ewr1], a[52:55] offset:1024\n\t"
    "ds_write_b128 %[ewr0], a[56:59] offset:2048\n\t"
    "ds_write_b128 %[ewr1], a[60:63] offset:3072\n\t"
    "v_mfma_f32_16x16x32_bf16 v[96:99], v[224:227], v[48:51], 0\n\t"
    "v_mfma_f32_16x16x32_bf16 v[100:103], v[228:231], v[48:51], 0\n\t"
    "v_mfma_f32_16x16x32_bf16 v[104:107], v[232:235], v[48:51], 0\n\t"
    "v_mfma_f32_16x16x32_bf16 v[108:111], v[236:239], v[48:51], 0\n\t"
    "v_mfma_f32_16x16x32_bf16 v[112:115], v[240:243], v[48:51], 0\n\t"
    "v_mfma_f32_16x16x32_bf16 v[116:119], v[244:247], v[48:51], 0\n\t"
    "v_mfma_f32_16x16x32_bf16 v[120:123], v[248:251], v[48:51], 0\n\t"
    "v_mfma_f32_16x16x32_bf16 v[124:127], v[252:255], v[48:51], 0\n\t"
    "s_nop 4\n\t"
    "s_mov_b32 exec_lo, 0x000f000f\n\t"
    "s_mov_b32 exec_hi, 0x000f000f\n\t"
    "v_cvt_pk_bf16_f32 v60, v96, v100\n\t"
    "v_cvt_pk_bf16_f32 v61, v104, v108\n\t"
    "v_cvt_pk_bf16_f32 v62, v112, v116\n\t"
    "v_cvt_pk_bf16_f32 v63, v120, v124\n\t"
    "s_mov_b32 exec_lo, 0x00f000f0\n\t"
    "s_mov_b32 exec_hi, 0x00f000f0\n\t"
    "v_cvt_pk_bf16_f32 v60, v97, v101\n\t"
    "v_cvt_pk_bf16_f32 v61, v105, v109\n\t"
    "v_cvt_pk_bf16_f32 v62, v113, v117\n\t"
    "v_cvt_pk_bf16_f32 v63, v121, v125\n\t"
    "s_mov_b32 exec_lo, 0x0f000f00\n\t"
    "s_mov_b32 exec_hi, 0x0f000f00\n\t"
    "v_cvt_pk_bf16_f32 v60, v98, v102\n\t"
    "v_cvt_pk_bf16_f32 v61, v106, v110\n\t"
    "v_cvt_pk_bf16_f32 v62, v114, v118\n\t"
    "v_cvt_pk_bf16_f32 v63, v122, v126\n\t"
    "s_mov_b32 exec_lo, 0xf000f000\n\t"
    "s_mov_b32 exec_hi, 0xf000f000\n\t"
    "v_cvt_pk_bf16_f32 v60, v99, v103\n\t"
    "v_cvt_pk_bf16_f32 v61, v107, v111\n\t"
    "v_cvt_pk_bf16_f32 v62, v115, v119\n\t"
    "v_cvt_pk_bf16_f32 v63, v123, v127\n\t"
    "s_mov_b64 exec, -1\n\t"
    "s_waitcnt lgkmcnt(4)\n\t"
    "s_nop 1\n\t"
    "v_mfma_f32_16x16x32_bf16 v[64:67], a[64:67], v[60:63], v[64:67]\n\t"
    "v_mfma_f32_16x16x32_bf16 v[68:71], a[68:71], v[60:63], v[68:71]\n\t"
    "v_mfma_f32_16x16x32_bf16 v[72:75], a[72:75], v[60:63], v[72:75]\n\t"
    "v_mfma_f32_16x16x32_bf16 v[76:79], a[76:79], v[60:63], v[76:79]\n\t"
    "buffer_load_dwordx4 v[224:227], %[vo0], %[rkv], %[so3] offen nt\n\t"
    "buffer_load_dwordx4 v[228:231], %[vo0], %[rkv], %[so3] offen offset:256 nt\n\t"
    "buffer_load_dwordx4 a[48:51], %[ef0], %[re4], %[se3] offen\n\t"
    "buffer_load_dwordx4 v[232:235], %[vo0], %[rkv], %[so3] offen offset:512 nt\n\t"
    "buffer_load_dwordx4 v[236:239], %[vo0], %[rkv], %[so3] offen offset:768 nt\n\t"
    "buffer_load_dwordx4 a[52:55], %[ef1], %[re4], %[se3] offen\n\t"
    "buffer_load_dwordx4 v[240:243], %[vo1], %[rkv], %[so3] offen nt\n\t"
    "buffer_load_dwordx4 v[244:247], %[vo1], %[rkv], %[so3] offen offset:256 nt\n\t"
    "buffer_load_dwordx4 a[56:59], %[ef2], %[re4], %[se3] offen\n\t"
    "buffer_load_dwordx4 v[248:251], %[vo1], %[rkv], %[so3] offen offset:512 nt\n\t"
    "buffer_load_dwordx4 v[252:255], %[vo1], %[rkv], %[so3] offen offset:768 nt\n\t"
    "buffer_load_dwordx4 a[60:63], %[ef3], %[re4], %[se3] offen\n\t"
          : RING_OPS("+"), "+{v[64:79]}"(sacc)
          : LOAD_OPS, "{v[48:51]}"(qb), [ewr0] "v"(ewr0), [ewr1] "v"(ewr1), [erd0] "v"(erd0), [erd1] "v"(erd1)
          : "memory", T_CLOB);
      TL(1 + i / 2);
    }
  }
  u32x8 pb;
  {
    asm volatile("s_nop 7" : "+v"(sacc));
#pragma unroll
    for (int i = 0; i < 16; ++i) { float x = sacc[i]; x += dpp<ROR4>(x); x += dpp<XOR8>(x); sacc[i] = x; }
    if (c < SQ) {
#pragma unroll
      for (int rt = 0; rt < 4; ++rt)
        *(LAS f32x4*)(lds + L_PART + w * 256 + c * 64 + rt * 16 + g * 4) = f32x4{sacc[4 * rt], sacc[4 * rt + 1], sacc[4 * rt + 2], sacc[4 * rt + 3]};
    }
    asm volatile("s_waitcnt lgkmcnt(0)" ::: "memory");
    __builtin_amdgcn_s_barrier();
    asm volatile("" ::: "memory");
    TL(5);
    LAS unsigned short* pl = (LAS unsigned short*)(lds + L_P + w * 128);
#pragma unroll
    for (int qq = 0; qq < SQ; ++qq) {
      float s = 0.f;
#pragma unroll
      for (int ww = 0; ww < NW; ++ww) s += lds[L_PART + ww * 256 + qq * 64 + lane];
      s *= 0.08838834764831845f;
      const float mx = wave_max(s);
      const float ex = __expf(s - mx);
      const float sum = wave_sum(ex);
      const float pv = ex / sum;
      pl[qq * 64 + lane] = (unsigned short)(cvtpk(pv, 0.f) & 0xffffu);
    }
    const u32x4 p0 = *(LAS u32x4*)(pl + (c >> 2) * 64 + 8 * g), p1 = *(LAS u32x4*)(pl + (c >> 2) * 64 + 32 + 8 * g);
    pb = u32x8{p0.x, p0.y, p0.z, p0.w, p1.x, p1.y, p1.z, p1.w};
  }
  TL(6);

  f32x32 oacc = 0.f;
  const unsigned trd0 = tile + (8 * g + (c >> 2)) * 128 + ((((c >> 1) & 1) | ((c >> 2) & 2) | ((g & 1) << 2)) << 4) + (c & 1) * 8;
  const unsigned trd1 = trd0 ^ 32, trd2 = trd0 ^ 64, trd3 = trd0 ^ 96;
#define V_OPS                                                                                             \
  "{v[40:47]}"(pb), [ewr0] "v"(ewr0), [ewr1] "v"(ewr1), [trd0] "v"(trd0), [trd1] "v"(trd1), [trd2] "v"(trd2), \
  [trd3] "v"(trd3), [sel0] "s"(0x05040100u), [sel1] "s"(0x07060302u)
#pragma nounroll
  for (int i = 0; i < NB - 2; i += 2) {
    const u32x4 rkv = mkdesc(vh, SKV * DH * 2);
    const u32x4 re = mkdesc(f_, RR * SKV * 2), re4 = mkdesc(f_, i + 4 < NB ? RR * SKV * 2 : 0);
    const int bA = BLK(i + 2), bB = BLK(i + 3), bC = BLK(i + 4);
    const unsigned vo0 = voV, vo1 = voV + 4096;
    asm volatile(
    "s_waitcnt vmcnt(36)\n\t"
    "ds_write_b128 %[ewr0], a[0:3] offset:4096\n\t"
    "ds_write_b128 %[ewr1], a[4:7] offset:5120\n\t"
    "ds_write_b128 %[ewr0], a[8:11] offset:6144\n\t"
    "ds_write_b128 %[ewr1], a[12:15] offset:7168\n\t"
    "ds_read_b64_tr_b16 a[64:65], %[trd0]\n\t"
    "ds_read_b64_tr_b16 a[66:67], %[trd0] offset:512\n\t"
    "ds_read_b64_tr_b16 a[68:69], %[trd0] offset:4096\n\t"
    "ds_read_b64_tr_b16 a[70:71], %[trd0] offset:4608\n\t"
    "ds_read_b64_tr_b16 a[72:73], %[trd1]\n\t"
    "ds_read_b64_tr_b16 a[74:75], %[trd1] offset:512\n\t"
    "ds_read_b64_tr_b16 a[76:77], %[trd1] offset:4096\n\t"
    "ds_read_b64_tr_b16 a[78:79], %[trd1] offset:4608\n\t"
    "v_perm_b32 v96, v132, v128, %[sel0]\n\t"
    "v_perm_b32 v97, v140, v136, %[sel0]\n\t"
    "v_perm_b32 v98, v148, v144, %[sel0]\n\t"
    "v_perm_b32 v99, v156, v152, %[sel0]\n\t"
    "v_perm_b32 v100, v132, v128, %[sel1]\n\t"
    "v_perm_b32 v101, v140, v136, %[sel1]\n\t"
    "v_perm_b32 v102, v148, v144, %[sel1]\n\t"
    "v_perm_b32 v103, v156, v152, %[sel1]\n\t"
    "v_perm_b32 v104, v133, v129, %[sel0]\n\t"
    "v_perm_b32 v105, v141, v137, %[sel0]\n\t"
    "v_perm_b32 v106, v149, v145, %[sel0]\n\t"
    "v_perm_b32 v107, v157, v153, %[sel0]\n\t"
    "v_perm_b32 v108, v133, v129, %[sel1]\n\t"
    "v_perm_b32 v109, v141, v137, %[sel1]\n\t"
    "v_perm_b32 v110, v149, v145, %[sel1]\n\t"
    "v_perm_b32 v111, v157, v153, %[sel1]\n\t"
    "s_waitcnt lgkmcnt(0)\n\t"
    "v_mfma_f32_16x16x32_bf16 v[52:55], a[64:67], v[40:43], 0\n\t"
    "v_mfma_f32_16x16x32_bf16 v[56:59], a[72:75], v[40:43], 0\n\t"
    "v_mfma_f32_16x16x32_bf16 v[52:55], a[68:71], v[44:47], v[52:55]\n\t"
    "v_mfma_f32_16x16x32_bf16 v[56:59], a[76:79], v[44:47], v[56:59]\n\t"
    "v_perm_b32 v112, v134, v130, %[sel0]\n\t"
    "v_perm_b32 v113, v142, v138, %[sel0]\n\t"
    "v_perm_b32 v114, v150, v146, %[sel0]\n\t"
    "v_perm_b32 v115, v158, v154, %[sel0]\n\t"
    "v_perm_b32 v116, v134, v130, %[sel1]\n\t"
    "v_perm_b32 v117, v142, v138, %[sel1]\n\t"
    "v_perm_b32 v118, v150, v146, %[sel1]\n\t"
    "v_perm_b32 v119, v158, v154, %[sel1]\n\t"
    "v_perm_b32 v120, v135, v131, %[sel0]\n\t"
    "v_perm_b32 v121, v143, v139, %[sel0]\n\t"
    "v_perm_b32 v122, v151, v147, %[sel0]\n\t"
    "v_perm_b32 v123, v159, v155, %[sel0]\n\t"
    "v_perm_b32 v124, v135, v131, %[sel1]\n\t"
    "v_perm_b32 v125, v143, v139, %[sel1]\n\t"
    "v_perm_b32 v126, v151, v147, %[sel1]\n\t"
    "v_perm_b32 v127, v159, v155, %[sel1]\n\t"
    "v_cvt_pk_bf16_f32 v60, v52, v53\n\t"
    "v_cvt_pk_bf16_f32 v61, v54, v55\n\t"
    "v_cvt_pk_bf16_f32 v62, v56, v57\n\t"
    "v_cvt_pk_bf16_f32 v63, v58, v59\n\t"
    "s_nop 1\n\t"
    "v_mfma_f32_16x16x32_bf16 v[64:67], v[60:63], v[96:99], v[64:67]\n\t"
    "v_mfma_f32_16x16x32_bf16 v[68:71], v[60:63], v[100:103], v[68:71]\n\t"
    "v_mfma_f32_16x16x32_bf16 v[72:75], v[60:63], v[104:107], v[72:75]\n\t"
    "v_mfma_f32_16x16x32_bf16 v[76:79], v[60:63], v[108:111], v[76:79]\n\t"
    "v_mfma_f32_16x16x32_bf16 v[80:83], v[60:63], v[112:115], v[80:83]\n\t"
    "v_mfma_f32_16x16x32_bf16 v[84:87], v[60:63], v[116:119], v[84:87]\n\t"
    "v_mfma_f32_16x16x32_bf16 v[88:91], v[60:63], v[120:123], v[88:91]\n\t"
    "v_mfma_f32_16x16x32_bf16 v[92:95], v[60:63], v[124:127], v[92:95]\n\t"
    "buffer_load_dwordx4 v[128:131], %[vo0], %[rkv], %[so0] offen nt\n\t"
    "buffer_load_dwordx4 v[132:135], %[vo0], %[rkv], %[so0] offen offset:256 nt\n\t"
    "buffer_load_dwordx4 a[0:3], %[ef0], %[re], %[se0] offen\n\t"
    "buffer_load_dwordx4 v[136:139], %[vo0], %[rkv], %[so0] offen offset:512 nt\n\t"
    "buffer_load_dwordx4 v[140:143], %[vo0], %[rkv], %[so0] offen offset:768 nt\n\t"
    "buffer_load_dwordx4 a[4:7], %[ef1], %[re], %[se0] offen\n\t"
    "buffer_load_dwordx4 v[144:147], %[vo1], %[rkv], %[so0] offen nt\n\t"
    "buffer_load_dwordx4 v[148:151], %[vo1], %[rkv], %[so0] offen offset:256 nt\n\t"
    "buffer_load_dwordx4 a[8:11], %[ef2], %[re], %[se0] offen\n\t"
    "buffer_load_dwordx4 v[152:155], %[vo1], %[rkv], %[so0] offen offset:512 nt\n\t"
    "buffer_load_dwordx4 v[156:159], %[vo1], %[rkv], %[so0] offen offset:768 nt\n\t"
    "buffer_load_dwordx4 a[12:15], %[ef3], %[re], %[se0] offen\n\t"
    "v_perm_b32 v120, v167, v163, %[sel0]\n\t"
    "v_perm_b32 v121, v175, v171, %[sel0]\n\t"
    "v_perm_b32 v122, v183, v179, %[sel0]\n\t"
    "v_perm_b32 v123, v191, v187, %[sel0]\n\t"
    "v_perm_b32 v124, v167, v163, %[sel1]\n\t"
    "v_perm_b32 v125, v175, v171, %[sel1]\n\t"
    "v_perm_b32 v126, v183, v179, %[sel1]\n\t"
    "v_perm_b32 v127, v191, v187, %[sel1]\n\t"
    "v_perm_b32 v112, v166, v162, %[sel0]\n\t"
    "v_perm_b32 v113, v174, v170, %[sel0]\n\t"
    "v_perm_b32 v114, v182, v178, %[sel0]\n\t"
    "v_perm_b32 v115, v190, v186, %[sel0]\n\t"
    "v_mfma_f32_16x16x32_bf16 v[72:75], v[60:63], v[104:107], v[72:75]\n\t"
    "v_perm_b32 v116, v166, v162, %[sel1]\n\t"
    "v_perm_b32 v117, v174, v170, %[sel1]\n\t"
    "v_perm_b32 v118, v182, v178, %[sel1]\n\t"
    "v_perm_b32 v119, v190, v186, %[sel1]\n\t"
    "v_mfma_f32_16x16x32_bf16 v[76:79], v[60:63], v[108:111], v[76:79]\n\t"
    "v_perm_b32 v120, v167, v163, %[sel0]\n\t"
    "v_perm_b32 v121, v175, v171, %[sel0]\n\t"
    "v_perm_b32 v122, v183, v179, %[sel0]\n\t"
    "v_perm_b32 v123, v191, v187, %[sel0]\n\t"
    "v_mfma_f32_16x16x32_bf16 v[80:83], v[60:63], v[112:115], v[80:83]\n\t"
    "v_perm_b32 v124, v167, v163, %[sel1]\n\t"
    "v_perm_b32 v125, v175, v171, %[sel1]\n\t"
    "v_perm_b32 v126, v183, v179, %[sel1]\n\t"
    "v_perm_b32 v127, v191, v187, %[sel1]\n\t"
    "v_mfma_f32_16x16x32_bf16 v[84:87], v[60:63], v[116:119], v[84:87]\n\t"
    "v_mfma_f32_16x16x32_bf16 v[88:91], v[60:63], v[120:123], v[88:91]\n\t"
    "v_mfma_f32_16x16x32_bf16 v[92:95], v[60:63], v[124:127], v[92:95]\n\t"
    "buffer_load_dwordx4 v[160:163], %[vo0], %[rkv], %[so1] offen nt\n\t"
    "buffer_load_dwordx4 v[164:167], %[vo0], %[rkv], %[so1] offen offset:256 nt\n\t"
    "buffer_load_dwordx4 a[16:19], %[ef0], %[re], %[se1] offen\n\t"
    "buffer_load_dwordx4 v[168:171], %[vo0], %[rkv], %[so1] offen offset:512 nt\n\t"
    "buffer_load_dwordx4 v[172:175], %[vo0], %[rkv], %[so1] offen offset:768 nt\n\t"
    "buffer_load_dwordx4 a[20:23], %[ef1], %[re], %[se1] offen\n\t"
    "buffer_load_dwordx4 v[176:179], %[vo1], %[rkv], %[so1] offen nt\n\t"
    "buffer_load_dwordx4 v[180:183], %[vo1], %[rkv], %[so1] offen offset:256 nt\n\t"
    "buffer_load_dwordx4 a[24:27], %[ef2], %[re], %[se1] offen\n\t"
    "buffer_load_dwordx4 v[184:187], %[vo1], %[rkv], %[so1] offen offset:512 nt\n\t"
    "buffer_load_dwordx4 v[188:191], %[vo1], %[rkv], %[so1] offen offset:768 nt\n\t"
    "buffer_load_dwordx4 a[28:31], %[ef3], %[re], %[se1] offen\n\t"
    "s_waitcnt vmcnt(36)\n\t"
    "ds_write_b128 %[ewr0], a[32:35] offset:4096\n\t"
    "ds_write_b128 %[ewr1], a[36:39] offset:5120\n\t"
    "ds_write_b128 %[ewr0], a[40:43] offset:6144\n\t"
    "ds_write_b128 %[ewr1], a[44:47] offset:7168\n\t"
    "ds_read_b64_tr_b16 a[64:65], %[trd0]\n\t"
    "ds_read_b64_tr_b16 a[66:67], %[trd0] offset:512\n\t"
    "ds_read_b64_tr_b16 a[68:69], %[trd0] offset:4096\n\t"
    "ds_read_b64_tr_b16 a[70:71], %[trd0] offset:4608\n\t"
    "ds_read_b64_tr_b16 a[72:73], %[trd1]\n\t"
    "ds_read_b64_tr_b16 a[74:75], %[trd1] offset:512\n\t"
    "ds_read_b64_tr_b16 a[76:77], %[trd1] offset:4096\n\t"
    "ds_read_b64_tr_b16 a[78:79], %[trd1] offset:4608\n\t"
    "v_perm_b32 v96, v196, v192, %[sel0]\n\t"
    "v_perm_b32 v97, v204, v200, %[sel0]\n\t"
    "v_perm_b32 v98, v212, v208, %[sel0]\n\t"
    "v_perm_b32 v99, v220, v216, %[sel0]\n\t"
    "v_perm_b32 v100, v196, v192, %[sel1]\n\t"
    "v_perm_b32 v101, v204, v200, %[sel1]\n\t"
    "v_perm_b32 v102, v212, v208, %[sel1]\n\t"
    "v_perm_b32 v103, v220, v216, %[sel1]\n\t"
    "v_perm_b32 v104, v197, v193, %[sel0]\n\t"
    "v_perm_b32 v105, v205, v201, %[sel0]\n\t"
    "v_perm_b32 v106, v213, v209, %[sel0]\n\t"
    "v_perm_b32 v107, v221, v217, %[sel0]\n\t"
    "v_perm_b32 v108, v197, v193, %[sel1]\n\t"
    "v_perm_b32 v109, v205, v201, %[sel1]\n\t"
    "v_perm_b32 v110, v213, v209, %[sel1]\n\t"
    "v_perm_b32 v111, v221, v217, %[sel1]\n\t"
    "s_waitcnt lgkmcnt(0)\n\t"
    "v_mfma_f32_16x16x32_bf16 v[52:55], a[64:67], v[40:43], 0\n\t"
    "v_mfma_f32_16x16x32_bf16 v[56:59], a[72:75], v[40:43], 0\n\t"
    "v_mfma_f32_16x16x32_bf16 v[52:55], a[68:71], v[44:47], v[52:55]\n\t"
    "v_mfma_f32_16x16x32_bf16 v[56:59], a[76:79], v[44:47], v[56:59]\n\t"
    "v_perm_b32 v112, v198, v194, %[sel0]\n\t"
    "v_perm_b32 v113, v206, v202, %[sel0]\n\t"
    "v_perm_b32 v114, v214, v210, %[sel0]\n\t"
    "v_perm_b32 v115, v222, v218, %[sel0]\n\t"
    "v_perm_b32 v116, v198, v194, %[sel1]\n\t"
    "v_perm_b32 v117, v206, v202, %[sel1]\n\t"
    "v_perm_b32 v118, v214, v210, %[sel1]\n\t"
    "v_perm_b32 v119, v222, v218, %[sel1]\n\t"
    "v_perm_b32 v120, v199, v195, %[sel0]\n\t"
    "v_perm_b32 v121, v207, v203, %[sel0]\n\t"
    "v_perm_b32 v122, v215, v211, %[sel0]\n\t"
    "v_perm_b32 v123, v223, v219, %[sel0]\n\t"
    "v_perm_b32 v124, v199, v195, %[sel1]\n\t"
    "v_perm_b32 v125, v207, v203, %[sel1]\n\t"
    "v_perm_b32 v126, v215, v211, %[sel1]\n\t"
    "v_perm_b32 v127, v223, v219, %[sel1]\n\t"
    "v_cvt_pk_bf16_f32 v60, v52, v53\n\t"
    "v_cvt_pk_bf16_f32 v61, v54, v55\n\t"
    "v_cvt_pk_bf16_f32 v62, v56, v57\n\t"
    "v_cvt_pk_bf16_f32 v63, v58, v59\n\t"
    "s_nop 1\n\t"
    "v_mfma_f32_16x16x32_bf16 v[64:67], v[60:63], v[96:99], v[64:67]\n\t"
    "v_mfma_f32_16x16x32_bf16 v[68:71], v[60:63], v[100:103], v[68:71]\n\t"
    "v_mfma_f32_16x16x32_bf16 v[72:75], v[60:63], v[104:107], v[72:75]\n\t"
    "v_mfma_f32_16x16x32_bf16 v[76:79], v[60:63], v[108:111], v[76:79]\n\t"
    "v_mfma_f32_16x16x32_bf16 v[80:83], v[60:63], v[112:115], v[80:83]\n\t"
    "v_mfma_f32_16x16x32_bf16 v[84:87], v[60:63], v[116:119], v[84:87]\n\t"
    "v_mfma_f32_16x16x32_bf16 v[88:91], v[60:63], v[120:123], v[88:91]\n\t"
    "v_mfma_f32_16x16x32_bf16 v[92:95], v[60:63], v[124:127], v[92:95]\n\t"
    "buffer_load_dwordx4 v[192:195], %[vo0], %[rkv], %[so2] offen nt\n\t"
    "buffer_load_dwordx4 v[196:199], %[vo0], %[rkv], %[so2] offen offset:256 nt\n\t"
    "buffer_load_dwordx4 a[32:35], %[ef0], %[re], %[se2] offen\n\t"
    "buffer_load_dwordx4 v[200:203], %[vo0], %[rkv], %[so2] offen offset:512 nt\n\t"
    "buffer_load_dwordx4 v[204:207], %[vo0], %[rkv], %[so2] offen offset:768 nt\n\t"
    "buffer_load_dwordx4 a[36:39], %[ef1], %[re], %[se2] offen\n\t"
    "buffer_load_dwordx4 v[208:211], %[vo1], %[rkv], %[so2] offen nt\n\t"
    "buffer_load_dwordx4 v[212:215], %[vo1], %[rkv], %[so2] offen offset:256 nt\n\t"
    "buffer_load_dwordx4 a[40:43], %[ef2], %[re], %[se2] offen\n\t"
    "buffer_load_dwordx4 v[216:219], %[vo1], %[rkv], %[so2] offen offset:512 nt\n\t"
    "buffer_load_dwordx4 v[220:223], %[vo1], %[rkv], %[so2] offen offset:768 nt\n\t"
    "buffer_load_dwordx4 a[44:47], %[ef3], %[re], %[se2] offen\n\t"
    "v_perm_b32 v120, v231, v227, %[sel0]\n\t"
    "v_perm_b32 v121, v239, v235, %[sel0]\n\t"
    "v_perm_b32 v122, v247, v243, %[sel0]\n\t"
    "v_perm_b32 v123, v255, v251, %[sel0]\n\t"
    "v_perm_b32 v124, v231, v227, %[sel1]\n\t"
    "v_perm_b32 v125, v239, v235, %[sel1]\n\t"
    "v_perm_b32 v126, v247, v243, %[sel1]\n\t"
    "v_perm_b32 v127, v255, v251, %[sel1]\n\t"
    "v_perm_b32 v112, v230, v226, %[sel0]\n\t"
    "v_perm_b32 v113, v238, v234, %[sel0]\n\t"
    "v_perm_b32 v114, v246, v242, %[sel0]\n\t"
    "v_perm_b32 v115, v254, v250, %[sel0]\n\t"
    "v_mfma_f32_16x16x32_bf16 v[72:75], v[60:63], v[104:107], v[72:75]\n\t"
    "v_perm_b32 v116, v230, v226, %[sel1]\n\t"
    "v_perm_b32 v117, v238, v234, %[sel1]\n\t"
    "v_perm_b32 v118, v246, v242, %[sel1]\n\t"
    "v_perm_b32 v119, v254, v250, %[sel1]\n\t"
    "v_mfma_f32_16x16x32_bf16 v[76:79], v[60:63], v[108:111], v[76:79]\n\t"
    "v_perm_b32 v120, v231, v227, %[sel0]\n\t"
    "v_perm_b32 v121, v239, v235, %[sel0]\n\t"
    "v_perm_b32 v122, v247, v243, %[sel0]\n\t"
    "v_perm_b32 v123, v255, v251, %[sel0]\n\t"
    "v_mfma_f32_16x16x32_bf16 v[80:83], v[60:63], v[112:115], v[80:83]\n\t"
    "v_perm_b32 v124, v231, v227, %[sel1]\n\t"
    "v_perm_b32 v125, v239, v235, %[sel1]\n\t"
    "v_perm_b32 v126, v247, v243, %[sel1]\n\t"
    "v_perm_b32 v127, v255, v251, %[sel1]\n\t"
    "v_mfma_f32_16x16x32_bf16 v[84:87], v[60:63], v[116:119], v[84:87]\n\t"
    "v_mfma_f32_16x16x32_bf16 v[88:91], v[60:63], v[120:123], v[88:91]\n\t"
    "v_mfma_f32_16x16x32_bf16 v[92:95], v[60:63], v[124:127], v[92:95]\n\t"
    "buffer_load_dwordx4 v[224:227], %[vo0], %[rkv], %[so3] offen nt\n\t"
    "buffer_load_dwordx4 v[228:231], %[vo0], %[rkv], %[so3] offen offset:256 nt\n\t"
    "buffer_load_dwordx4 a[48:51], %[ef0], %[re4], %[se3] offen\n\t"
    "buffer_load_dwordx4 v[232:235], %[vo0], %[rkv], %[so3] offen offset:512 nt\n\t"
    "buffer_load_dwordx4 v[236:239], %[vo0], %[rkv], %[so3] offen offset:768 nt\n\t"
    "buffer_load_dwordx4 a[52:55], %[ef1], %[re4], %[se3] offen\n\t"
    "buffer_load_dwordx4 v[240:243], %[vo1], %[rkv], %[so3] offen nt\n\t"
    "buffer_load_dwordx4 v[244:247], %[vo1], %[rkv], %[so3] offen offset:256 nt\n\t"
    "buffer_load_dwordx4 a[56:59], %[ef2], %[re4], %[se3] offen\n\t"
    "buffer_load_dwordx4 v[248:251], %[vo1], %[rkv], %[so3] offen offset:512 nt\n\t"
    "buffer_load_dwordx4 v[252:255], %[vo1], %[rkv], %[so3] offen offset:768 nt\n\t"
    "buffer_load_dwordx4 a[60:63], %[ef3], %[re4], %[se3] offen\n\t"
        : RING_OPS("+"), "+{v[64:95]}"(oacc)
        : LOAD_OPS, V_OPS
        : "memory", T_CLOB);
    TL(7 + i / 2);
  }
  asm volatile(
    "s_waitcnt vmcnt(36)\n\t"
    "ds_write_b128 %[ewr0], a[0:3] offset:4096\n\t"
    "ds_write_b128 %[ewr1], a[4:7] offset:5120\n\t"
    "ds_write_b128 %[ewr0], a[8:11] offset:6144\n\t"
    "ds_write_b128 %[ewr1], a[12:15] offset:7168\n\t"
    "ds_read_b64_tr_b16 a[64:65], %[trd0]\n\t"
    "ds_read_b64_tr_b16 a[66:67], %[trd0] offset:512\n\t"
    "ds_read_b64_tr_b16 a[68:69], %[trd0] offset:4096\n\t"
    "ds_read_b64_tr_b16 a[70:71], %[trd0] offset:4608\n\t"
    "ds_read_b64_tr_b16 a[72:73], %[trd1]\n\t"
    "ds_read_b64_tr_b16 a[74:75], %[trd1] offset:512\n\t"
    "ds_read_b64_tr_b16 a[76:77], %[trd1] offset:4096\n\t"
    "ds_read_b64_tr_b16 a[78:79], %[trd1] offset:4608\n\t"
    "v_perm_b32 v96, v132, v128, %[sel0]\n\t"
    "v_perm_b32 v97, v140, v136, %[sel0]\n\t"
    "v_perm_b32 v98, v148, v144, %[sel0]\n\t"
    "v_perm_b32 v99, v156, v152, %[sel0]\n\t"
    "v_perm_b32 v100, v132, v128, %[sel1]\n\t"
    "v_perm_b32 v101, v140, v136, %[sel1]\n\t"
    "v_perm_b32 v102, v148, v144, %[sel1]\n\t"
    "v_perm_b32 v103, v156, v152, %[sel1]\n\t"
    "v_perm_b32 v104, v133, v129, %[sel0]\n\t"
    "v_perm_b32 v105, v141, v137, %[sel0]\n\t"
    "v_perm_b32 v106, v149, v145, %[sel0]\n\t"
    "v_perm_b32 v107, v157, v153, %[sel0]\n\t"
    "v_perm_b32 v108, v133, v129, %[sel1]\n\t"
    "v_perm_b32 v109, v141, v137, %[sel1]\n\t"
    "v_perm_b32 v110, v149, v145, %[sel1]\n\t"
    "v_perm_b32 v111, v157, v153, %[sel1]\n\t"
    "s_waitcnt lgkmcnt(0)\n\t"
    "v_mfma_f32_16x16x32_bf16 v[52:55], a[64:67], v[40:43], 0\n\t"
    "v_mfma_f32_16x16x32_bf16 v[56:59], a[72:75], v[40:43], 0\n\t"
    "v_mfma_f32_16x16x32_bf16 v[52:55], a[68:71], v[44:47], v[52:55]\n\t"
    "v_mfma_f32_16x16x32_bf16 v[56:59], a[76:79], v[44:47], v[56:59]\n\t"
    "v_perm_b32 v112, v134, v130, %[sel0]\n\t"
    "v_perm_b32 v113, v142, v138, %[sel0]\n\t"
    "v_perm_b32 v114, v150, v146, %[sel0]\n\t"
    "v_perm_b32 v115, v158, v154, %[sel0]\n\t"
    "v_perm_b32 v116, v134, v130, %[sel1]\n\t"
    "v_perm_b32 v117, v142, v138, %[sel1]\n\t"
    "v_perm_b32 v118, v150, v146, %[sel1]\n\t"
    "v_perm_b32 v119, v158, v154, %[sel1]\n\t"
    "v_perm_b32 v120, v135, v131, %[sel0]\n\t"
    "v_perm_b32 v121, v143, v139, %[sel0]\n\t"
    "v_perm_b32 v122, v151, v147, %[sel0]\n\t"
    "v_perm_b32 v123, v159, v155, %[sel0]\n\t"
    "v_perm_b32 v124, v135, v131, %[sel1]\n\t"
    "v_perm_b32 v125, v143, v139, %[sel1]\n\t"
    "v_perm_b32 v126, v151, v147, %[sel1]\n\t"
    "v_perm_b32 v127, v159, v155, %[sel1]\n\t"
    "v_cvt_pk_bf16_f32 v60, v52, v53\n\t"
    "v_cvt_pk_bf16_f32 v61, v54, v55\n\t"
    "v_cvt_pk_bf16_f32 v62, v56, v57\n\t"
    "v_cvt_pk_bf16_f32 v63, v58, v59\n\t"
    "s_nop 1\n\t"
    "v_mfma_f32_16x16x32_bf16 v[64:67], v[60:63], v[96:99], v[64:67]\n\t"
    "v_mfma_f32_16x16x32_bf16 v[68:71], v[60:63], v[100:103], v[68:71]\n\t"
    "v_mfma_f32_16x16x32_bf16 v[72:75], v[60:63], v[104:107], v[72:75]\n\t"
    "v_mfma_f32_16x16x32_bf16 v[76:79], v[60:63], v[108:111], v[76:79]\n\t"
    "v_mfma_f32_16x16x32_bf16 v[80:83], v[60:63], v[112:115], v[80:83]\n\t"
    "v_mfma_f32_16x16x32_bf16 v[84:87], v[60:63], v[116:119], v[84:87]\n\t"
    "v_mfma_f32_16x16x32_bf16 v[88:91], v[60:63], v[120:123], v[88:91]\n\t"
    "v_mfma_f32_16x16x32_bf16 v[92:95], v[60:63], v[124:127], v[92:95]\n\t"
    "v_perm_b32 v120, v167, v163, %[sel0]\n\t"
    "v_perm_b32 v121, v175, v171, %[sel0]\n\t"
    "v_perm_b32 v122, v183, v179, %[sel0]\n\t"
    "v_perm_b32 v123, v191, v187, %[sel0]\n\t"
    "v_perm_b32 v124, v167, v163, %[sel1]\n\t"
    "v_perm_b32 v125, v175, v171, %[sel1]\n\t"
    "v_perm_b32 v126, v183, v179, %[sel1]\n\t"
    "v_perm_b32 v127, v191, v187, %[sel1]\n\t"
    "v_perm_b32 v112, v166, v162, %[sel0]\n\t"
    "v_perm_b32 v113, v174, v170, %[sel0]\n\t"
    "v_perm_b32 v114, v182, v178, %[sel0]\n\t"
    "v_perm_b32 v115, v190, v186, %[sel0]\n\t"
    "v_mfma_f32_16x16x32_bf16 v[72:75], v[60:63], v[104:107], v[72:75]\n\t"
    "v_perm_b32 v116, v166, v162, %[sel1]\n\t"
    "v_perm_b32 v117, v174, v170, %[sel1]\n\t"
    "v_perm_b32 v118, v182, v178, %[sel1]\n\t"
    "v_perm_b32 v119, v190, v186, %[sel1]\n\t"
    "v_mfma_f32_16x16x32_bf16 v[76:79], v[60:63], v[108:111], v[76:79]\n\t"
    "v_perm_b32 v120, v167, v163, %[sel0]\n\t"
    "v_perm_b32 v121, v175, v171, %[sel0]\n\t"
    "v_perm_b32 v122, v183, v179, %[sel0]\n\t"
    "v_perm_b32 v123, v191, v187, %[sel0]\n\t"
    "v_mfma_f32_16x16x32_bf16 v[80:83], v[60:63], v[112:115], v[80:83]\n\t"
    "v_perm_b32 v124, v167, v163, %[sel1]\n\t"
    "v_perm_b32 v125, v175, v171, %[sel1]\n\t"
    "v_perm_b32 v126, v183, v179, %[sel1]\n\t"
    "v_perm_b32 v127, v191, v187, %[sel1]\n\t"
    "v_mfma_f32_16x16x32_bf16 v[84:87], v[60:63], v[116:119], v[84:87]\n\t"
    "v_mfma_f32_16x16x32_bf16 v[88:91], v[60:63], v[120:123], v[88:91]\n\t"
    "v_mfma_f32_16x16x32_bf16 v[92:95], v[60:63], v[124:127], v[92:95]\n\t"
    "s_waitcnt vmcnt(12)\n\t"
    "ds_write_b128 %[ewr0], a[32:35] offset:4096\n\t"
    "ds_write_b128 %[ewr1], a[36:39] offset:5120\n\t"
    "ds_write_b128 %[ewr0], a[40:43] offset:6144\n\t"
    "ds_write_b128 %[ewr1], a[44:47] offset:7168\n\t"
    "ds_read_b64_tr_b16 a[64:65], %[trd0]\n\t"
    "ds_read_b64_tr_b16 a[66:67], %[trd0] offset:512\n\t"
    "ds_read_b64_tr_b16 a[68:69], %[trd0] offset:4096\n\t"
    "ds_read_b64_tr_b16 a[70:71], %[trd0] offset:4608\n\t"
    "ds_read_b64_tr_b16 a[72:73], %[trd1]\n\t"
    "ds_read_b64_tr_b16 a[74:75], %[trd1] offset:512\n\t"
    "ds_read_b64_tr_b16 a[76:77], %[trd1] offset:4096\n\t"
    "ds_read_b64_tr_b16 a[78:79], %[trd1] offset:4608\n\t"
    "v_perm_b32 v96, v196, v192, %[sel0]\n\t"
    "v_perm_b32 v97, v204, v200, %[sel0]\n\t"
    "v_perm_b32 v98, v212, v208, %[sel0]\n\t"
    "v_perm_b32 v99, v220, v216, %[sel0]\n\t"
    "v_perm_b32 v100, v196, v192, %[sel1]\n\t"
    "v_perm_b32 v101, v204, v200, %[sel1]\n\t"
    "v_perm_b32 v102, v212, v208, %[sel1]\n\t"
    "v_perm_b32 v103, v220, v216, %[sel1]\n\t"
    "v_perm_b32 v104, v197, v193, %[sel0]\n\t"
    "v_perm_b32 v105, v205, v201, %[sel0]\n\t"
    "v_perm_b32 v106, v213, v209, %[sel0]\n\t"
    "v_perm_b32 v107, v221, v217, %[sel0]\n\t"
    "v_perm_b32 v108, v197, v193, %[sel1]\n\t"
    "v_perm_b32 v109, v205, v201, %[sel1]\n\t"
    "v_perm_b32 v110, v213, v209, %[sel1]\n\t"
    "v_perm_b32 v111, v221, v217, %[sel1]\n\t"
    "s_waitcnt lgkmcnt(0)\n\t"
    "v_mfma_f32_16x16x32_bf16 v[52:55], a[64:67], v[40:43], 0\n\t"
    "v_mfma_f32_16x16x32_bf16 v[56:59], a[72:75], v[40:43], 0\n\t"
    "v_mfma_f32_16x16x32_bf16 v[52:55], a[68:71], v[44:47], v[52:55]\n\t"
    "v_mfma_f32_16x16x32_bf16 v[56:59], a[76:79], v[44:47], v[56:59]\n\t"
    "v_perm_b32 v112, v198, v194, %[sel0]\n\t"
    "v_perm_b32 v113, v206, v202, %[sel0]\n\t"
    "v_perm_b32 v114, v214, v210, %[sel0]\n\t"
    "v_perm_b32 v115, v222, v218, %[sel0]\n\t"
    "v_perm_b32 v116, v198, v194, %[sel1]\n\t"
    "v_perm_b32 v117, v206, v202, %[sel1]\n\t"
    "v_perm_b32 v118, v214, v210, %[sel1]\n\t"
    "v_perm_b32 v119, v222, v218, %[sel1]\n\t"
    "v_perm_b32 v120, v199, v195, %[sel0]\n\t"
    "v_perm_b32 v121, v207, v203, %[sel0]\n\t"
    "v_perm_b32 v122, v215, v211, %[sel0]\n\t"
    "v_perm_b32 v123, v223, v219, %[sel0]\n\t"
    "v_perm_b32 v124, v199, v195, %[sel1]\n\t"
    "v_perm_b32 v125, v207, v203, %[sel1]\n\t"
    "v_perm_b32 v126, v215, v211, %[sel1]\n\t"
    "v_perm_b32 v127, v223, v219, %[sel1]\n\t"
    "v_cvt_pk_bf16_f32 v60, v52, v53\n\t"
    "v_cvt_pk_bf16_f32 v61, v54, v55\n\t"
    "v_cvt_pk_bf16_f32 v62, v56, v57\n\t"
    "v_cvt_pk_bf16_f32 v63, v58, v59\n\t"
    "s_nop 1\n\t"
    "v_mfma_f32_16x16x32_bf16 v[64:67], v[60:63], v[96:99], v[64:67]\n\t"
    "v_mfma_f32_16x16x32_bf16 v[68:71], v[60:63], v[100:103], v[68:71]\n\t"
    "v_mfma_f32_16x16x32_bf16 v[72:75], v[60:63], v[104:107], v[72:75]\n\t"
    "v_mfma_f32_16x16x32_bf16 v[76:79], v[60:63], v[108:111], v[76:79]\n\t"
    "v_mfma_f32_16x16x32_bf16 v[80:83], v[60:63], v[112:115], v[80:83]\n\t"
    "v_mfma_f32_16x16x32_bf16 v[84:87], v[60:63], v[116:119], v[84:87]\n\t"
    "v_mfma_f32_16x16x32_bf16 v[88:91], v[60:63], v[120:123], v[88:91]\n\t"
    "v_mfma_f32_16x16x32_bf16 v[92:95], v[60:63], v[124:127], v[92:95]\n\t"
    "v_perm_b32 v120, v231, v227, %[sel0]\n\t"
    "v_perm_b32 v121, v239, v235, %[sel0]\n\t"
    "v_perm_b32 v122, v247, v243, %[sel0]\n\t"
    "v_perm_b32 v123, v255, v251, %[sel0]\n\t"
    "v_perm_b32 v124, v231, v227, %[sel1]\n\t"
    "v_perm_b32 v125, v239, v235, %[sel1]\n\t"
    "v_perm_b32 v126, v247, v243, %[sel1]\n\t"
    "v_perm_b32 v127, v255, v251, %[sel1]\n\t"
    "v_perm_b32 v112, v230, v226, %[sel0]\n\t"
    "v_perm_b32 v113, v238, v234, %[sel0]\n\t"
    "v_perm_b32 v114, v246, v242, %[sel0]\n\t"
    "v_perm_b32 v115, v254, v250, %[sel0]\n\t"
    "v_mfma_f32_16x16x32_bf16 v[72:75], v[60:63], v[104:107], v[72:75]\n\t"
    "v_perm_b32 v116, v230, v226, %[sel1]\n\t"
    "v_perm_b32 v117, v238, v234, %[sel1]\n\t"
    "v_perm_b32 v118, v246, v242, %[sel1]\n\t"
    "v_perm_b32 v119, v254, v250, %[sel1]\n\t"
    "v_mfma_f32_16x16x32_bf16 v[76:79], v[60:63], v[108:111], v[76:79]\n\t"
    "v_perm_b32 v120, v231, v227, %[sel0]\n\t"
    "v_perm_b32 v121, v239, v235, %[sel0]\n\t"
    "v_perm_b32 v122, v247, v243, %[sel0]\n\t"
    "v_perm_b32 v123, v255, v251, %[sel0]\n\t"
    "v_mfma_f32_16x16x32_bf16 v[80:83], v[60:63], v[112:115], v[80:83]\n\t"
    "v_perm_b32 v124, v231, v227, %[sel1]\n\t"
    "v_perm_b32 v125, v239, v235, %[sel1]\n\t"
    "v_perm_b32 v126, v247, v243, %[sel1]\n\t"
    "v_perm_b32 v127, v255, v251, %[sel1]\n\t"
    "v_mfma_f32_16x16x32_bf16 v[84:87], v[60:63], v[116:119], v[84:87]\n\t"
    "v_mfma_f32_16x16x32_bf16 v[88:91], v[60:63], v[120:123], v[88:91]\n\t"
    "v_mfma_f32_16x16x32_bf16 v[92:95], v[60:63], v[124:127], v[92:95]\n\t"
      "s_nop 7"
      : RING_OPS("+"), "+{v[64:95]}"(oacc)
      : V_OPS
      : "memory", T_CLOB);
  TL(10);
#pragma unroll
  for (int cc = 0; cc < 8; ++cc) lds[L_O + w * 512 + cc * 64 + lane] = oacc[4 * cc];
  asm volatile("s_waitcnt lgkmcnt(0)" ::: "memory");
  __builtin_amdgcn_s_barrier();
  asm volatile("" ::: "memory");
  {
    float s0 = 0.f, s1 = 0.f;
#pragma unroll
    for (int ww = 0; ww < NW; ++ww) {
      s0 += lds[L_O + ww * 512 + (2 * w) * 64 + lane];
      s1 += lds[L_O + ww * 512 + (2 * w + 1) * 64 + lane];
    }
    *(GAS unsigned*)((GAS char*)out_ + (size_t)head * (SQ * DH * 2) + lane * 16 + w * 4) = cvtpk(s0, s1);
  }
  TL(11);
}

}

extern "C" void kernel_launch(void* const* d_in, const int* in_sizes, int n_in, void* d_out, int out_size,
                              void* d_ws, size_t ws_size, hipStream_t stream) {
  (void)in_sizes; (void)n_in; (void)out_size; (void)d_ws; (void)ws_size;
  lin::linformer_kernel<<<lin::NHEAD, lin::NW * 64, 0, stream>>>((const char*)d_in[0], (const char*)d_in[1], (const char*)d_in[2],
                                                           (const char*)d_in[3], (const char*)d_in[4], (char*)d_out
      );
}
```

```cpp
#include <hip/hip_runtime.h>
#include <hip/hip_bf16.h>
#include <stdint.h>

namespace lin {

constexpr int NW = 2;
constexpr int SQ = 4, SKV = 2048, DH = 128, RR = 64;
constexpr int NHEAD = 256;
constexpr int NB = SKV / 64 / NW;

#define GAS __attribute__((address_space(1)))
#define LAS __attribute__((address_space(3)))
typedef unsigned u32x2 __attribute__((ext_vector_type(2)));
typedef unsigned u32x4 __attribute__((ext_vector_type(4)));
typedef unsigned u32x8 __attribute__((ext_vector_type(8)));
typedef unsigned u32x32 __attribute__((ext_vector_type(32)));
typedef float f32x2 __attribute__((ext_vector_type(2)));
typedef float f32x16 __attribute__((ext_vector_type(16)));
typedef float f32x32 __attribute__((ext_vector_type(32)));
typedef __bf16 bf16x2 __attribute__((ext_vector_type(2)));

__device__ __forceinline__ u32x4 mkdesc(const char* p, unsigned bytes) {
  const unsigned long long a = (unsigned long long)p;
  return u32x4{(unsigned)a, (unsigned)(a >> 32) & 0xffffu, bytes, 0x00020000u};
}
__device__ __forceinline__ unsigned cvtpk(float lo, float hi) {
  f32x2 v = {lo, hi};
  bf16x2 r = __builtin_convertvector(v, bf16x2);
  return __builtin_bit_cast(unsigned, r);
}

constexpr int L_E = 0;
constexpr int L_PART = L_E + NW * 2048;
constexpr int L_O = L_PART + NW * 256;
constexpr int L_TOTAL = 21 * 1024;

#define RING_OPS                                                                                          \
  "+{v[128:159]}"(r0), "+{v[160:191]}"(r1), "+{v[192:223]}"(r2), "+{v[224:255]}"(r3),                       \
  "+{a[0:31]}"(e01), "+{a[32:63]}"(e23)
#define LOAD_OPS                                                                                          \
  [vo0] "v"(vo0), [vo1] "v"(vo1), [ef0] "v"(ef0), [ef1] "v"(ef1), [ef2] "v"(ef2), [ef3] "v"(ef3),           \
  [rkv] "s"(rkv), [re] "s"(re), [re4] "s"(re4), [so0] "s"(bA * 16384), [so1] "s"(bA * 16384 + 8192),       \
  [so2] "s"(bB * 16384), [so3] "s"(bB * 16384 + 8192), [se0] "s"(bA * 128 + 131072), [se1] "s"(bB * 128),  \
  [se2] "s"(bB * 128 + 131072), [se3] "s"(bC * 128)
#define T_CLOB                                                                                            \
  "v52", "v53", "v54", "v55", "v56", "v57", "v58", "v59", "v60", "v61", "v62", "v63",                      \
  "v96", "v97", "v98", "v99", "v100", "v101", "v102", "v103", "v104", "v105", "v106", "v107", "v108",      \
  "v109", "v110", "v111", "v112", "v113", "v114", "v115", "v116", "v117", "v118", "v119", "v120", "v121",  \
  "v122", "v123", "v124", "v125", "v126", "v127", "a64", "a65", "a66", "a67", "a68", "a69", "a70", "a71",  \
  "a72", "a73", "a74", "a75", "a76", "a77", "a78", "a79"
#define BLK(n) (w + NW * (((n) & (NB - 1)) ^ bx))

__global__ void __launch_bounds__(NW * 64, 1)
linformer_kernel(const char* __restrict__ q_, const char* __restrict__ k_, const char* __restrict__ v_,
                 const char* __restrict__ e_, const char* __restrict__ f_, char* __restrict__ out_) {
  __shared__ __attribute__((aligned(256))) float lds_raw[L_TOTAL];
  LAS float* lds = (LAS float*)lds_raw;
  const unsigned lbase = (unsigned)(unsigned long long)lds;
  const int bid = blockIdx.x;
  const int head = (bid & 7) * 32 + (bid >> 3);
  const int tid = threadIdx.x;
  const int lane = tid & 63;
  const int w = __builtin_amdgcn_readfirstlane(tid >> 6);
  const int c = lane & 15, g = lane >> 4;
  const int bx = ((head & 1) ? 15 : 0) ^ ((head & 2) ? 5 : 0);

  const char* kh = k_ + (size_t)head * (SKV * DH * 2);
  const char* vh = v_ + (size_t)head * (SKV * DH * 2);
  const unsigned voK = (c >> 2) * 2048 + g * 64 + (c & 3) * 16;
  u32x32 r0, r1, r2, r3, e01, e23;
  {
    asm volatile(
    "buffer_load_dwordx4 v[128:131], %[vo0], %[rkv], %[so0] offen nt\n\t"
    "buffer_load_dwordx4 v[132:135], %[vo0], %[rkv], %[so0] offen offset:256 nt\n\t"
    "buffer_load_dwordx4 v[136:139], %[vo0], %[rkv], %[so0] offen offset:512 nt\n\t"
    "buffer_load_dwordx4 v[140:143], %[vo0], %[rkv], %[so0] offen offset:768 nt\n\t"
    "buffer_load_dwordx4 v[144:147], %[vo1], %[rkv], %[so0] offen nt\n\t"
    "buffer_load_dwordx4 v[148:151], %[vo1], %[rkv], %[so0] offen offset:256 nt\n\t"
    "buffer_load_dwordx4 v[152:155], %[vo1], %[rkv], %[so0] offen offset:512 nt\n\t"
    "buffer_load_dwordx4 v[156:159], %[vo1], %[rkv], %[so0] offen offset:768 nt\n\t"
        : "=&{v[128:159]}"(r0)
        : [vo0] "v"(voK), [vo1] "v"(voK + 1024), [rkv] "s"(mkdesc(kh, SKV * DH * 2)), [so0] "s"(BLK(0) * 16384)
        : "memory");
    __builtin_amdgcn_sched_barrier(0);
  }
  const unsigned voV = g * 1024 + c * 16;
  const unsigned ef0 = (lane >> 3) * (SKV * 2) + (lane & 7) * 16;
  const unsigned ef1 = ef0 + 32768, ef2 = ef0 + 65536, ef3 = ef0 + 98304;
  const unsigned tile = lbase + w * 8192;
  const unsigned ewr0 = tile + (lane >> 3) * 128 + (((lane & 7) ^ ((lane >> 3) & 2)) << 4), ewr1 = ewr0 ^ 64;

  u32x4 qb;
  f32x16 sacc;
  f32x32 oacc;
  u32x8 pb;

  {
    const u32x4 rkv = mkdesc(kh, SKV * DH * 2), re = mkdesc(e_, RR * SKV * 2), re4 = re;
    const int bA = BLK(0), bB = BLK(1), bC = BLK(2);
    const unsigned vo0 = voK, vo1 = voK + 1024;
    asm volatile(
    "global_load_dwordx4 v[48:51], %[qoff], %[qb]\n\t"
    "buffer_load_dwordx4 a[80:83], %[ef0], %[re], %[seP] offen\n\t"
    "buffer_load_dwordx4 a[84:87], %[ef1], %[re], %[seP] offen\n\t"
    "buffer_load_dwordx4 a[88:91], %[ef2], %[re], %[seP] offen\n\t"
    "buffer_load_dwordx4 a[92:95], %[ef3], %[re], %[seP] offen\n\t"
    "buffer_load_dwordx4 a[0:3], %[ef0], %[re], %[se0] offen\n\t"
    "buffer_load_dwordx4 a[4:7], %[ef1], %[re], %[se0] offen\n\t"
    "buffer_load_dwordx4 a[8:11], %[ef2], %[re], %[se0] offen\n\t"
    "buffer_load_dwordx4 a[12:15], %[ef3], %[re], %[se0] offen\n\t"
    "buffer_load_dwordx4 v[160:163], %[vo0], %[rkv], %[so1] offen nt\n\t"
    "buffer_load_dwordx4 v[164:167], %[vo0], %[rkv], %[so1] offen offset:256 nt\n\t"
    "buffer_load_dwordx4 a[16:19], %[ef0], %[re], %[se1] offen\n\t"
    "buffer_load_dwordx4 v[168:171], %[vo0], %[rkv], %[so1] offen offset:512 nt\n\t"
    "buffer_load_dwordx4 v[172:175], %[vo0], %[rkv], %[so1] offen offset:768 nt\n\t"
    "buffer_load_dwordx4 a[20:23], %[ef1], %[re], %[se1] offen\n\t"
    "buffer_load_dwordx4 v[176:179], %[vo1], %[rkv], %[so1] offen nt\n\t"
    "buffer_load_dwordx4 v[180:183], %[vo1], %[rkv], %[so1] offen offset:256 nt\n\t"
    "buffer_load_dwordx4 a[24:27], %[ef2], %[re], %[se1] offen\n\t"
    "buffer_load_dwordx4 v[184:187], %[vo1], %[rkv], %[so1] offen offset:512 nt\n\t"
    "buffer_load_dwordx4 v[188:191], %[vo1], %[rkv], %[so1] offen offset:768 nt\n\t"
    "buffer_load_dwordx4 a[28:31], %[ef3], %[re], %[se1] offen\n\t"
    "buffer_load_dwordx4 v[192:195], %[vo0], %[rkv], %[so2] offen nt\n\t"
    "buffer_load_dwordx4 v[196:199], %[vo0], %[rkv], %[so2] offen offset:256 nt\n\t"
    "buffer_load_dwordx4 a[32:35], %[ef0], %[re], %[se2] offen\n\t"
    "buffer_load_dwordx4 v[200:203], %[vo0], %[rkv], %[so2] offen offset:512 nt\n\t"
    "buffer_load_dwordx4 v[204:207], %[vo0], %[rkv], %[so2] offen offset:768 nt\n\t"
    "buffer_load_dwordx4 a[36:39], %[ef1], %[re], %[se2] offen\n\t"
    "buffer_load_dwordx4 v[208:211], %[vo1], %[rkv], %[so2] offen nt\n\t"
    "buffer_load_dwordx4 v[212:215], %[vo1], %[rkv], %[so2] offen offset:256 nt\n\t"
    "buffer_load_dwordx4 a[40:43], %[ef2], %[re], %[se2] offen\n\t"
    "buffer_load_dwordx4 v[216:219], %[vo1], %[rkv], %[so2] offen offset:512 nt\n\t"
    "buffer_load_dwordx4 v[220:223], %[vo1], %[rkv], %[so2] offen offset:768 nt\n\t"
    "buffer_load_dwordx4 a[44:47], %[ef3], %[re], %[se2] offen\n\t"
    "buffer_load_dwordx4 v[224:227], %[vo0], %[rkv], %[so3] offen nt\n\t"
    "buffer_load_dwordx4 v[228:231], %[vo0], %[rkv], %[so3] offen offset:256 nt\n\t"
    "buffer_load_dwordx4 a[48:51], %[ef0], %[re4], %[se3] offen\n\t"
    "buffer_load_dwordx4 v[232:235], %[vo0], %[rkv], %[so3] offen offset:512 nt\n\t"
    "buffer_load_dwordx4 v[236:239], %[vo0], %[rkv], %[so3] offen offset:768 nt\n\t"
    "buffer_load_dwordx4 a[52:55], %[ef1], %[re4], %[se3] offen\n\t"
    "buffer_load_dwordx4 v[240:243], %[vo1], %[rkv], %[so3] offen nt\n\t"
    "buffer_load_dwordx4 v[244:247], %[vo1], %[rkv], %[so3] offen offset:256 nt\n\t"
    "buffer_load_dwordx4 a[56:59], %[ef2], %[re4], %[se3] offen\n\t"
    "buffer_load_dwordx4 v[248:251], %[vo1], %[rkv], %[so3] offen offset:512 nt\n\t"
    "buffer_load_dwordx4 v[252:255], %[vo1], %[rkv], %[so3] offen offset:768 nt\n\t"
    "buffer_load_dwordx4 a[60:63], %[ef3], %[re4], %[se3] offen\n\t"
    "v_mov_b32 v64, 0\n\t"
    "v_mov_b32 v65, 0\n\t"
    "v_mov_b32 v66, 0\n\t"
    "v_mov_b32 v67, 0\n\t"
    "v_mov_b32 v68, 0\n\t"
    "v_mov_b32 v69, 0\n\t"
    "v_mov_b32 v70, 0\n\t"
    "v_mov_b32 v71, 0\n\t"
    "v_mov_b32 v72, 0\n\t"
    "v_mov_b32 v73, 0\n\t"
    "v_mov_b32 v74, 0\n\t"
    "v_mov_b32 v75, 0\n\t"
    "v_mov_b32 v76, 0\n\t"
    "v_mov_b32 v77, 0\n\t"
    "v_mov_b32 v78, 0\n\t"
    "v_mov_b32 v79, 0\n\t"
    "s_waitcnt vmcnt(40)\n\t"
    "ds_write_b128 %[ewr0], a[80:83]\n\t"
    "ds_write_b128 %[ewr1], a[84:87] offset:1024\n\t"
    "ds_write_b128 %[ewr0], a[88:91] offset:2048\n\t"
    "ds_write_b128 %[ewr1], a[92:95] offset:3072\n\t"
        : "+{v[128:159]}"(r0), "=&{v[160:191]}"(r1), "=&{v[192:223]}"(r2), "=&{v[224:255]}"(r3), "=&{a[0:31]}"(e01),
          "=&{a[32:63]}"(e23), "=&{v[48:51]}"(qb), "=&{v[64:79]}"(sacc)
        : LOAD_OPS, [ewr0] "v"(ewr0), [ewr1] "v"(ewr1), [seP] "s"(bA * 128),
          [qb] "s"(q_ + (size_t)head * (SQ * DH * 2)), [qoff] "v"((c & 3) * 256 + g * 64 + (c >> 2) * 16)
        : "memory", "a80", "a81", "a82", "a83", "a84", "a85", "a86", "a87", "a88", "a89", "a90", "a91", "a92", "a93",
          "a94", "a95");
  }

  {
    const unsigned erd0 = tile + c * 128 + ((g ^ (c & 2) ^ ((c & 8) >> 1)) << 4), erd1 = erd0 ^ 64;
#pragma nounroll
    for (int i = 0; i < NB - 2; i += 2) {
      const u32x4 rkv = mkdesc(kh, SKV * DH * 2);
      const u32x4 re = mkdesc(e_, RR * SKV * 2), re4 = mkdesc(i + 4 < NB ? e_ : f_, RR * SKV * 2);
      const int bA = BLK(i + 2), bB = BLK(i + 3), bC = BLK(i + 4);
      const unsigned vo0 = voK, vo1 = voK + 1024;
      asm volatile(
    "s_waitcnt vmcnt(36)\n\t"
    "ds_write_b128 %[ewr0], a[0:3] offset:4096\n\t"
    "ds_write_b128 %[ewr1], a[4:7] offset:5120\n\t"
    "ds_write_b128 %[ewr0], a[8:11] offset:6144\n\t"
    "ds_write_b128 %[ewr1], a[12:15] offset:7168\n\t"
    "ds_read_b128 a[64:67], %[erd0]\n\t"
    "ds_read_b128 a[68:71], %[erd0] offset:2048\n\t"
    "ds_read_b128 a[72:75], %[erd0] offset:4096\n\t"
    "ds_read_b128 a[76:79], %[erd0] offset:6144\n\t"
    "v_mfma_f32_16x16x32_bf16 v[96:99], v[128:131], v[48:51], 0\n\t"
    "v_mfma_f32_16x16x32_bf16 v[100:103], v[132:135], v[48:51], 0\n\t"
    "v_mfma_f32_16x16x32_bf16 v[104:107], v[136:139], v[48:51], 0\n\t"
    "v_mfma_f32_16x16x32_bf16 v[108:111], v[140:143], v[48:51], 0\n\t"
    "v_mfma_f32_16x16x32_bf16 v[112:115], v[144:147], v[48:51], 0\n\t"
    "v_mfma_f32_16x16x32_bf16 v[116:119], v[148:151], v[48:51], 0\n\t"
    "v_mfma_f32_16x16x32_bf16 v[120:123], v[152:155], v[48:51], 0\n\t"
    "v_mfma_f32_16x16x32_bf16 v[124:127], v[156:159], v[48:51], 0\n\t"
    "s_nop 4\n\t"
    "s_mov_b32 exec_lo, 0x000f000f\n\t"
    "s_mov_b32 exec_hi, 0x000f000f\n\t"
    "v_cvt_pk_bf16_f32 v60, v96, v100\n\t"
    "v_cvt_pk_bf16_f32 v61, v104, v108\n\t"
    "v_cvt_pk_bf16_f32 v62, v112, v116\n\t"
    "v_cvt_pk_bf16_f32 v63, v120, v124\n\t"
    "s_mov_b32 exec_lo, 0x00f000f0\n\t"
    "s_mov_b32 exec_hi, 0x00f000f0\n\t"
    "v_cvt_pk_bf16_f32 v60, v97, v101\n\t"
    "v_cvt_pk_bf16_f32 v61, v105, v109\n\t"
    "v_cvt_pk_bf16_f32 v62, v113, v117\n\t"
    "v_cvt_pk_bf16_f32 v63, v121, v125\n\t"
    "s_mov_b32 exec_lo, 0x0f000f00\n\t"
    "s_mov_b32 exec_hi, 0x0f000f00\n\t"
    "v_cvt_pk_bf16_f32 v60, v98, v102\n\t"
    "v_cvt_pk_bf16_f32 v61, v106, v110\n\t"
    "v_cvt_pk_bf16_f32 v62, v114, v118\n\t"
    "v_cvt_pk_bf16_f32 v63, v122, v126\n\t"
    "s_mov_b32 exec_lo, 0xf000f000\n\t"
    "s_mov_b32 exec_hi, 0xf000f000\n\t"
    "v_cvt_pk_bf16_f32 v60, v99, v103\n\t"
    "v_cvt_pk_bf16_f32 v61, v107, v111\n\t"
    "v_cvt_pk_bf16_f32 v62, v115, v119\n\t"
    "v_cvt_pk_bf16_f32 v63, v123, v127\n\t"
    "s_mov_b64 exec, -1\n\t"
    "s_waitcnt lgkmcnt(0)\n\t"
    "s_nop 1\n\t"
    "v_mfma_f32_16x16x32_bf16 v[64:67], a[64:67], v[60:63], v[64:67]\n\t"
    "v_mfma_f32_16x16x32_bf16 v[68:71], a[68:71], v[60:63], v[68:71]\n\t"
    "v_mfma_f32_16x16x32_bf16 v[72:75], a[72:75], v[60:63], v[72:75]\n\t"
    "v_mfma_f32_16x16x32_bf16 v[76:79], a[76:79], v[60:63], v[76:79]\n\t"
    "buffer_load_dwordx4 v[128:131], %[vo0], %[rkv], %[so0] offen nt\n\t"
    "buffer_load_dwordx4 v[132:135], %[vo0], %[rkv], %[so0] offen offset:256 nt\n\t"
    "buffer_load_dwordx4 a[0:3], %[ef0], %[re], %[se0] offen\n\t"
    "buffer_load_dwordx4 v[136:139], %[vo0], %[rkv], %[so0] offen offset:512 nt\n\t"
    "buffer_load_dwordx4 v[140:143], %[vo0], %[rkv], %[so0] offen offset:768 nt\n\t"
    "buffer_load_dwordx4 a[4:7], %[ef1], %[re], %[se0] offen\n\t"
    "buffer_load_dwordx4 v[144:147], %[vo1], %[rkv], %[so0] offen nt\n\t"
    "buffer_load_dwordx4 v[148:151], %[vo1], %[rkv], %[so0] offen offset:256 nt\n\t"
    "buffer_load_dwordx4 a[8:11], %[ef2], %[re], %[se0] offen\n\t"
    "buffer_load_dwordx4 v[152:155], %[vo1], %[rkv], %[so0] offen offset:512 nt\n\t"
    "buffer_load_dwordx4 v[156:159], %[vo1], %[rkv], %[so0] offen offset:768 nt\n\t"
    "buffer_load_dwordx4 a[12:15], %[ef3], %[re], %[se0] offen\n\t"
    "s_waitcnt vmcnt(36)\n\t"
    "ds_read_b128 a[64:67], %[erd1]\n\t"
    "ds_read_b128 a[68:71], %[erd1] offset:2048\n\t"
    "ds_read_b128 a[72:75], %[erd1] offset:4096\n\t"
    "ds_read_b128 a[76:79], %[erd1] offset:6144\n\t"
    "ds_write_b128 %[ewr0], a[16:19]\n\t"
    "ds_write_b128 %[ewr1], a[20:23] offset:1024\n\t"
    "ds_write_b128 %[ewr0], a[24:27] offset:2048\n\t"
    "ds_write_b128 %[ewr1], a[28:31] offset:3072\n\t"
    "v_mfma_f32_16x16x32_bf16 v[96:99], v[160:163], v[48:51], 0\n\t"
    "v_mfma_f32_16x16x32_bf16 v[100:103], v[164:167], v[48:51], 0\n\t"
    "v_mfma_f32_16x16x32_bf16 v[104:107], v[168:171], v[48:51], 0\n\t"
    "v_mfma_f32_16x16x32_bf16 v[108:111], v[172:175], v[48:51], 0\n\t"
    "v_mfma_f32_16x16x32_bf16 v[112:115], v[176:179], v[48:51], 0\n\t"
    "v_mfma_f32_16x16x32_bf16 v[116:119], v[180:183], v[48:51], 0\n\t"
    "v_mfma_f32_16x16x32_bf16 v[120:123], v[184:187], v[48:51], 0\n\t"
    "v_mfma_f32_16x16x32_bf16 v[124:127], v[188:191], v[48:51], 0\n\t"
    "s_nop 4\n\t"
    "s_mov_b32 exec_lo, 0x000f000f\n\t"
    "s_mov_b32 exec_hi, 0x000f000f\n\t"
    "v_cvt_pk_bf16_f32 v60, v96, v100\n\t"
    "v_cvt_pk_bf16_f32 v61, v104, v108\n\t"
    "v_cvt_pk_bf16_f32 v62, v112, v116\n\t"
    "v_cvt_pk_bf16_f32 v63, v120, v124\n\t"
    "s_mov_b32 exec_lo, 0x00f000f0\n\t"
    "s_mov_b32 exec_hi, 0x00f000f0\n\t"
    "v_cvt_pk_bf16_f32 v60, v97, v101\n\t"
    "v_cvt_pk_bf16_f32 v61, v105, v109\n\t"
    "v_cvt_pk_bf16_f32 v62, v113, v117\n\t"
    "v_cvt_pk_bf16_f32 v63, v121, v125\n\t"
    "s_mov_b32 exec_lo, 0x0f000f00\n\t"
    "s_mov_b32 exec_hi, 0x0f000f00\n\t"
    "v_cvt_pk_bf16_f32 v60, v98, v102\n\t"
    "v_cvt_pk_bf16_f32 v61, v106, v110\n\t"
    "v_cvt_pk_bf16_f32 v62, v114, v118\n\t"
    "v_cvt_pk_bf16_f32 v63, v122, v126\n\t"
    "s_mov_b32 exec_lo, 0xf000f000\n\t"
    "s_mov_b32 exec_hi, 0xf000f000\n\t"
    "v_cvt_pk_bf16_f32 v60, v99, v103\n\t"
    "v_cvt_pk_bf16_f32 v61, v107, v111\n\t"
    "v_cvt_pk_bf16_f32 v62, v115, v119\n\t"
    "v_cvt_pk_bf16_f32 v63, v123, v127\n\t"
    "s_mov_b64 exec, -1\n\t"
    "s_waitcnt lgkmcnt(0)\n\t"
    "s_nop 1\n\t"
    "v_mfma_f32_16x16x32_bf16 v[64:67], a[64:67], v[60:63], v[64:67]\n\t"
    "v_mfma_f32_16x16x32_bf16 v[68:71], a[68:71], v[60:63], v[68:71]\n\t"
    "v_mfma_f32_16x16x32_bf16 v[72:75], a[72:75], v[60:63], v[72:75]\n\t"
    "v_mfma_f32_16x16x32_bf16 v[76:79], a[76:79], v[60:63], v[76:79]\n\t"
    "buffer_load_dwordx4 v[160:163], %[vo0], %[rkv], %[so1] offen nt\n\t"
    "buffer_load_dwordx4 v[164:167], %[vo0], %[rkv], %[so1] offen offset:256 nt\n\t"
    "buffer_load_dwordx4 a[16:19], %[ef0], %[re], %[se1] offen\n\t"
    "buffer_load_dwordx4 v[168:171], %[vo0], %[rkv], %[so1] offen offset:512 nt\n\t"
    "buffer_load_dwordx4 v[172:175], %[vo0], %[rkv], %[so1] offen offset:768 nt\n\t"
    "buffer_load_dwordx4 a[20:23], %[ef1], %[re], %[se1] offen\n\t"
    "buffer_load_dwordx4 v[176:179], %[vo1], %[rkv], %[so1] offen nt\n\t"
    "buffer_load_dwordx4 v[180:183], %[vo1], %[rkv], %[so1] offen offset:256 nt\n\t"
    "buffer_load_dwordx4 a[24:27], %[ef2], %[re], %[se1] offen\n\t"
    "buffer_load_dwordx4 v[184:187], %[vo1], %[rkv], %[so1] offen offset:512 nt\n\t"
    "buffer_load_dwordx4 v[188:191], %[vo1], %[rkv], %[so1] offen offset:768 nt\n\t"
    "buffer_load_dwordx4 a[28:31], %[ef3], %[re], %[se1] offen\n\t"
    "s_waitcnt vmcnt(36)\n\t"
    "ds_write_b128 %[ewr0], a[32:35] offset:4096\n\t"
    "ds_write_b128 %[ewr1], a[36:39] offset:5120\n\t"
    "ds_write_b128 %[ewr0], a[40:43] offset:6144\n\t"
    "ds_write_b128 %[ewr1], a[44:47] offset:7168\n\t"
    "ds_read_b128 a[64:67], %[erd0]\n\t"
    "ds_read_b128 a[68:71], %[erd0] offset:2048\n\t"
    "ds_read_b128 a[72:75], %[erd0] offset:4096\n\t"
    "ds_read_b128 a[76:79], %[erd0] offset:6144\n\t"
    "v_mfma_f32_16x16x32_bf16 v[96:99], v[192:195], v[48:51], 0\n\t"
    "v_mfma_f32_16x16x32_bf16 v[100:103], v[196:199], v[48:51], 0\n\t"
    "v_mfma_f32_16x16x32_bf16 v[104:107], v[200:203], v[48:51], 0\n\t"
    "v_mfma_f32_16x16x32_bf16 v[108:111], v[204:207], v[48:51], 0\n\t"
    "v_mfma_f32_16x16x32_bf16 v[112:115], v[208:211], v[48:51], 0\n\t"
    "v_mfma_f32_16x16x32_bf16 v[116:119], v[212:215], v[48:51], 0\n\t"
    "v_mfma_f32_16x16x32_bf16 v[120:123], v[216:219], v[48:51], 0\n\t"
    "v_mfma_f32_16x16x32_bf16 v[124:127], v[220:223], v[48:51], 0\n\t"
    "s_nop 4\n\t"
    "s_mov_b32 exec_lo, 0x000f000f\n\t"
    "s_mov_b32 exec_hi, 0x000f000f\n\t"
    "v_cvt_pk_bf16_f32 v60, v96, v100\n\t"
    "v_cvt_pk_bf16_f32 v61, v104, v108\n\t"
    "v_cvt_pk_bf16_f32 v62, v112, v116\n\t"
    "v_cvt_pk_bf16_f32 v63, v120, v124\n\t"
    "s_mov_b32 exec_lo, 0x00f000f0\n\t"
    "s_mov_b32 exec_hi, 0x00f000f0\n\t"
    "v_cvt_pk_bf16_f32 v60, v97, v101\n\t"
    "v_cvt_pk_bf16_f32 v61, v105, v109\n\t"
    "v_cvt_pk_bf16_f32 v62, v113, v117\n\t"
    "v_cvt_pk_bf16_f32 v63, v121, v125\n\t"
    "s_mov_b32 exec_lo, 0x0f000f00\n\t"
    "s_mov_b32 exec_hi, 0x0f000f00\n\t"
    "v_cvt_pk_bf16_f32 v60, v98, v102\n\t"
    "v_cvt_pk_bf16_f32 v61, v106, v110\n\t"
    "v_cvt_pk_bf16_f32 v62, v114, v118\n\t"
    "v_cvt_pk_bf16_f32 v63, v122, v126\n\t"
    "s_mov_b32 exec_lo, 0xf000f000\n\t"
    "s_mov_b32 exec_hi, 0xf000f000\n\t"
    "v_cvt_pk_bf16_f32 v60, v99, v103\n\t"
    "v_cvt_pk_bf16_f32 v61, v107, v111\n\t"
    "v_cvt_pk_bf16_f32 v62, v115, v119\n\t"
    "v_cvt_pk_bf16_f32 v63, v123, v127\n\t"
    "s_mov_b64 exec, -1\n\t"
    "s_waitcnt lgkmcnt(0)\n\t"
    "s_nop 1\n\t"
    "v_mfma_f32_16x16x32_bf16 v[64:67], a[64:67], v[60:63], v[64:67]\n\t"
    "v_mfma_f32_16x16x32_bf16 v[68:71], a[68:71], v[60:63], v[68:71]\n\t"
    "v_mfma_f32_16x16x32_bf16 v[72:75], a[72:75], v[60:63], v[72:75]\n\t"
    "v_mfma_f32_16x16x32_bf16 v[76:79], a[76:79], v[60:63], v[76:79]\n\t"
    "buffer_load_dwordx4 v[192:195], %[vo0], %[rkv], %[so2] offen nt\n\t"
    "buffer_load_dwordx4 v[196:199], %[vo0], %[rkv], %[so2] offen offset:256 nt\n\t"
    "buffer_load_dwordx4 a[32:35], %[ef0], %[re], %[se2] offen\n\t"
    "buffer_load_dwordx4 v[200:203], %[vo0], %[rkv], %[so2] offen offset:512 nt\n\t"
    "buffer_load_dwordx4 v[204:207], %[vo0], %[rkv], %[so2] offen offset:768 nt\n\t"
    "buffer_load_dwordx4 a[36:39], %[ef1], %[re], %[se2] offen\n\t"
    "buffer_load_dwordx4 v[208:211], %[vo1], %[rkv], %[so2] offen nt\n\t"
    "buffer_load_dwordx4 v[212:215], %[vo1], %[rkv], %[so2] offen offset:256 nt\n\t"
    "buffer_load_dwordx4 a[40:43], %[ef2], %[re], %[se2] offen\n\t"
    "buffer_load_dwordx4 v[216:219], %[vo1], %[rkv], %[so2] offen offset:512 nt\n\t"
    "buffer_load_dwordx4 v[220:223], %[vo1], %[rkv], %[so2] offen offset:768 nt\n\t"
    "buffer_load_dwordx4 a[44:47], %[ef3], %[re], %[se2] offen\n\t"
    "s_waitcnt vmcnt(36)\n\t"
    "ds_read_b128 a[64:67], %[erd1]\n\t"
    "ds_read_b128 a[68:71], %[erd1] offset:2048\n\t"
    "ds_read_b128 a[72:75], %[erd1] offset:4096\n\t"
    "ds_read_b128 a[76:79], %[erd1] offset:6144\n\t"
    "ds_write_b128 %[ewr0], a[48:51]\n\t"
    "ds_write_b128 %[ewr1], a[52:55] offset:1024\n\t"
    "ds_write_b128 %[ewr0], a[56:59] offset:2048\n\t"
    "ds_write_b128 %[ewr1], a[60:63] offset:3072\n\t"
    "v_mfma_f32_16x16x32_bf16 v[96:99], v[224:227], v[48:51], 0\n\t"
    "v_mfma_f32_16x16x32_bf16 v[100:103], v[228:231], v[48:51], 0\n\t"
    "v_mfma_f32_16x16x32_bf16 v[104:107], v[232:235], v[48:51], 0\n\t"
    "v_mfma_f32_16x16x32_bf16 v[108:111], v[236:239], v[48:51], 0\n\t"
    "v_mfma_f32_16x16x32_bf16 v[112:115], v[240:243], v[48:51], 0\n\t"
    "v_mfma_f32_16x16x32_bf16 v[116:119], v[244:247], v[48:51], 0\n\t"
    "v_mfma_f32_16x16x32_bf16 v[120:123], v[248:251], v[48:51], 0\n\t"
    "v_mfma_f32_16x16x32_bf16 v[124:127], v[252:255], v[48:51], 0\n\t"
    "s_nop 4\n\t"
    "s_mov_b32 exec_lo, 0x000f000f\n\t"
    "s_mov_b32 exec_hi, 0x000f000f\n\t"
    "v_cvt_pk_bf16_f32 v60, v96, v100\n\t"
    "v_cvt_pk_bf16_f32 v61, v104, v108\n\t"
    "v_cvt_pk_bf16_f32 v62, v112, v116\n\t"
    "v_cvt_pk_bf16_f32 v63, v120, v124\n\t"
    "s_mov_b32 exec_lo, 0x00f000f0\n\t"
    "s_mov_b32 exec_hi, 0x00f000f0\n\t"
    "v_cvt_pk_bf16_f32 v60, v97, v101\n\t"
    "v_cvt_pk_bf16_f32 v61, v105, v109\n\t"
    "v_cvt_pk_bf16_f32 v62, v113, v117\n\t"
    "v_cvt_pk_bf16_f32 v63, v121, v125\n\t"
    "s_mov_b32 exec_lo, 0x0f000f00\n\t"
    "s_mov_b32 exec_hi, 0x0f000f00\n\t"
    "v_cvt_pk_bf16_f32 v60, v98, v102\n\t"
    "v_cvt_pk_bf16_f32 v61, v106, v110\n\t"
    "v_cvt_pk_bf16_f32 v62, v114, v118\n\t"
    "v_cvt_pk_bf16_f32 v63, v122, v126\n\t"
    "s_mov_b32 exec_lo, 0xf000f000\n\t"
    "s_mov_b32 exec_hi, 0xf000f000\n\t"
    "v_cvt_pk_bf16_f32 v60, v99, v103\n\t"
    "v_cvt_pk_bf16_f32 v61, v107, v111\n\t"
    "v_cvt_pk_bf16_f32 v62, v115, v119\n\t"
    "v_cvt_pk_bf16_f32 v63, v123, v127\n\t"
    "s_mov_b64 exec, -1\n\t"
    "s_waitcnt lgkmcnt(0)\n\t"
    "s_nop 1\n\t"
    "v_mfma_f32_16x16x32_bf16 v[64:67], a[64:67], v[60:63], v[64:67]\n\t"
    "v_mfma_f32_16x16x32_bf16 v[68:71], a[68:71], v[60:63], v[68:71]\n\t"
    "v_mfma_f32_16x16x32_bf16 v[72:75], a[72:75], v[60:63], v[72:75]\n\t"
    "v_mfma_f32_16x16x32_bf16 v[76:79], a[76:79], v[60:63], v[76:79]\n\t"
    "buffer_load_dwordx4 v[224:227], %[vo0], %[rkv], %[so3] offen nt\n\t"
    "buffer_load_dwordx4 v[228:231], %[vo0], %[rkv], %[so3] offen offset:256 nt\n\t"
    "buffer_load_dwordx4 a[48:51], %[ef0], %[re4], %[se3] offen\n\t"
    "buffer_load_dwordx4 v[232:235], %[vo0], %[rkv], %[so3] offen offset:512 nt\n\t"
    "buffer_load_dwordx4 v[236:239], %[vo0], %[rkv], %[so3] offen offset:768 nt\n\t"
    "buffer_load_dwordx4 a[52:55], %[ef1], %[re4], %[se3] offen\n\t"
    "buffer_load_dwordx4 v[240:243], %[vo1], %[rkv], %[so3] offen nt\n\t"
    "buffer_load_dwordx4 v[244:247], %[vo1], %[rkv], %[so3] offen offset:256 nt\n\t"
    "buffer_load_dwordx4 a[56:59], %[ef2], %[re4], %[se3] offen\n\t"
    "buffer_load_dwordx4 v[248:251], %[vo1], %[rkv], %[so3] offen offset:512 nt\n\t"
    "buffer_load_dwordx4 v[252:255], %[vo1], %[rkv], %[so3] offen offset:768 nt\n\t"
    "buffer_load_dwordx4 a[60:63], %[ef3], %[re4], %[se3] offen\n\t"
          : RING_OPS, "+{v[64:79]}"(sacc)
          : LOAD_OPS, "{v[48:51]}"(qb), [ewr0] "v"(ewr0), [ewr1] "v"(ewr1), [erd0] "v"(erd0), [erd1] "v"(erd1)
          : "memory", T_CLOB);
    }
    {
      const u32x4 rkv = mkdesc(vh, SKV * DH * 2), re = mkdesc(f_, RR * SKV * 2), re4 = re;
      const int bA = BLK(0), bB = BLK(1), bC = BLK(2);
      const unsigned vo0 = voV, vo1 = voV + 4096;
      const unsigned pwr = lbase + L_PART * 4 + w * 1024 + (c & 3) * 256 + g * 16;
      const unsigned prd = lbase + L_PART * 4 + (c >> 2) * 256 + (c & 2) * 64 + g * 32 + (c & 1) * 16;
      f32x16 ohi;
      asm volatile(
    "s_waitcnt vmcnt(36)\n\t"
    "ds_write_b128 %[ewr0], a[0:3] offset:4096\n\t"
    "ds_write_b128 %[ewr1], a[4:7] offset:5120\n\t"
    "ds_write_b128 %[ewr0], a[8:11] offset:6144\n\t"
    "ds_write_b128 %[ewr1], a[12:15] offset:7168\n\t"
    "ds_read_b128 a[64:67], %[erd0]\n\t"
    "ds_read_b128 a[68:71], %[erd0] offset:2048\n\t"
    "ds_read_b128 a[72:75], %[erd0] offset:4096\n\t"
    "ds_read_b128 a[76:79], %[erd0] offset:6144\n\t"
    "v_mfma_f32_16x16x32_bf16 v[96:99], v[128:131], v[48:51], 0\n\t"
    "v_mfma_f32_16x16x32_bf16 v[100:103], v[132:135], v[48:51], 0\n\t"
    "v_mfma_f32_16x16x32_bf16 v[104:107], v[136:139], v[48:51], 0\n\t"
    "v_mfma_f32_16x16x32_bf16 v[108:111], v[140:143], v[48:51], 0\n\t"
    "v_mfma_f32_16x16x32_bf16 v[112:115], v[144:147], v[48:51], 0\n\t"
    "v_mfma_f32_16x16x32_bf16 v[116:119], v[148:151], v[48:51], 0\n\t"
    "v_mfma_f32_16x16x32_bf16 v[120:123], v[152:155], v[48:51], 0\n\t"
    "v_mfma_f32_16x16x32_bf16 v[124:127], v[156:159], v[48:51], 0\n\t"
    "s_nop 4\n\t"
    "s_mov_b32 exec_lo, 0x000f000f\n\t"
    "s_mov_b32 exec_hi, 0x000f000f\n\t"
    "v_cvt_pk_bf16_f32 v60, v96, v100\n\t"
    "v_cvt_pk_bf16_f32 v61, v104, v108\n\t"
    "v_cvt_pk_bf16_f32 v62, v112, v116\n\t"
    "v_cvt_pk_bf16_f32 v63, v120, v124\n\t"
    "s_mov_b32 exec_lo, 0x00f000f0\n\t"
    "s_mov_b32 exec_hi, 0x00f000f0\n\t"
    "v_cvt_pk_bf16_f32 v60, v97, v101\n\t"
    "v_cvt_pk_bf16_f32 v61, v105, v109\n\t"
    "v_cvt_pk_bf16_f32 v62, v113, v117\n\t"
    "v_cvt_pk_bf16_f32 v63, v121, v125\n\t"
    "s_mov_b32 exec_lo, 0x0f000f00\n\t"
    "s_mov_b32 exec_hi, 0x0f000f00\n\t"
    "v_cvt_pk_bf16_f32 v60, v98, v102\n\t"
    "v_cvt_pk_bf16_f32 v61, v106, v110\n\t"
    "v_cvt_pk_bf16_f32 v62, v114, v118\n\t"
    "v_cvt_pk_bf16_f32 v63, v122, v126\n\t"
    "s_mov_b32 exec_lo, 0xf000f000\n\t"
    "s_mov_b32 exec_hi, 0xf000f000\n\t"
    "v_cvt_pk_bf16_f32 v60, v99, v103\n\t"
    "v_cvt_pk_bf16_f32 v61, v107, v111\n\t"
    "v_cvt_pk_bf16_f32 v62, v115, v119\n\t"
    "v_cvt_pk_bf16_f32 v63, v123, v127\n\t"
    "s_mov_b64 exec, -1\n\t"
    "s_waitcnt lgkmcnt(0)\n\t"
    "s_nop 1\n\t"
    "v_mfma_f32_16x16x32_bf16 v[64:67], a[64:67], v[60:63], v[64:67]\n\t"
    "v_mfma_f32_16x16x32_bf16 v[68:71], a[68:71], v[60:63], v[68:71]\n\t"
    "v_mfma_f32_16x16x32_bf16 v[72:75], a[72:75], v[60:63], v[72:75]\n\t"
    "v_mfma_f32_16x16x32_bf16 v[76:79], a[76:79], v[60:63], v[76:79]\n\t"
    "buffer_load_dwordx4 v[128:131], %[vo0], %[rkv], %[so0] offen nt\n\t"
    "buffer_load_dwordx4 v[132:135], %[vo0], %[rkv], %[so0] offen offset:256 nt\n\t"
    "buffer_load_dwordx4 a[0:3], %[ef0], %[re], %[se0] offen\n\t"
    "buffer_load_dwordx4 v[136:139], %[vo0], %[rkv], %[so0] offen offset:512 nt\n\t"
    "buffer_load_dwordx4 v[140:143], %[vo0], %[rkv], %[so0] offen offset:768 nt\n\t"
    "buffer_load_dwordx4 a[4:7], %[ef1], %[re], %[se0] offen\n\t"
    "buffer_load_dwordx4 v[144:147], %[vo1], %[rkv], %[so0] offen nt\n\t"
    "buffer_load_dwordx4 v[148:151], %[vo1], %[rkv], %[so0] offen offset:256 nt\n\t"
    "buffer_load_dwordx4 a[8:11], %[ef2], %[re], %[se0] offen\n\t"
    "buffer_load_dwordx4 v[152:155], %[vo1], %[rkv], %[so0] offen offset:512 nt\n\t"
    "buffer_load_dwordx4 v[156:159], %[vo1], %[rkv], %[so0] offen offset:768 nt\n\t"
    "buffer_load_dwordx4 a[12:15], %[ef3], %[re], %[se0] offen\n\t"
    "s_waitcnt vmcnt(36)\n\t"
    "ds_read_b128 a[64:67], %[erd1]\n\t"
    "ds_read_b128 a[68:71], %[erd1] offset:2048\n\t"
    "ds_read_b128 a[72:75], %[erd1] offset:4096\n\t"
    "ds_read_b128 a[76:79], %[erd1] offset:6144\n\t"
    "ds_write_b128 %[ewr0], a[16:19]\n\t"
    "ds_write_b128 %[ewr1], a[20:23] offset:1024\n\t"
    "ds_write_b128 %[ewr0], a[24:27] offset:2048\n\t"
    "ds_write_b128 %[ewr1], a[28:31] offset:3072\n\t"
    "v_mfma_f32_16x16x32_bf16 v[96:99], v[160:163], v[48:51], 0\n\t"
    "v_mfma_f32_16x16x32_bf16 v[100:103], v[164:167], v[48:51], 0\n\t"
    "v_mfma_f32_16x16x32_bf16 v[104:107], v[168:171], v[48:51], 0\n\t"
    "v_mfma_f32_16x16x32_bf16 v[108:111], v[172:175], v[48:51], 0\n\t"
    "v_mfma_f32_16x16x32_bf16 v[112:115], v[176:179], v[48:51], 0\n\t"
    "v_mfma_f32_16x16x32_bf16 v[116:119], v[180:183], v[48:51], 0\n\t"
    "v_mfma_f32_16x16x32_bf16 v[120:123], v[184:187], v[48:51], 0\n\t"
    "v_mfma_f32_16x16x32_bf16 v[124:127], v[188:191], v[48:51], 0\n\t"
    "s_nop 4\n\t"
    "s_mov_b32 exec_lo, 0x000f000f\n\t"
    "s_mov_b32 exec_hi, 0x000f000f\n\t"
    "v_cvt_pk_bf16_f32 v60, v96, v100\n\t"
    "v_cvt_pk_bf16_f32 v61, v104, v108\n\t"
    "v_cvt_pk_bf16_f32 v62, v112, v116\n\t"
    "v_cvt_pk_bf16_f32 v63, v120, v124\n\t"
    "s_mov_b32 exec_lo, 0x00f000f0\n\t"
    "s_mov_b32 exec_hi, 0x00f000f0\n\t"
    "v_cvt_pk_bf16_f32 v60, v97, v101\n\t"
    "v_cvt_pk_bf16_f32 v61, v105, v109\n\t"
    "v_cvt_pk_bf16_f32 v62, v113, v117\n\t"
    "v_cvt_pk_bf16_f32 v63, v121, v125\n\t"
    "s_mov_b32 exec_lo, 0x0f000f00\n\t"
    "s_mov_b32 exec_hi, 0x0f000f00\n\t"
    "v_cvt_pk_bf16_f32 v60, v98, v102\n\t"
    "v_cvt_pk_bf16_f32 v61, v106, v110\n\t"
    "v_cvt_pk_bf16_f32 v62, v114, v118\n\t"
    "v_cvt_pk_bf16_f32 v63, v122, v126\n\t"
    "s_mov_b32 exec_lo, 0xf000f000\n\t"
    "s_mov_b32 exec_hi, 0xf000f000\n\t"
    "v_cvt_pk_bf16_f32 v60, v99, v103\n\t"
    "v_cvt_pk_bf16_f32 v61, v107, v111\n\t"
    "v_cvt_pk_bf16_f32 v62, v115, v119\n\t"
    "v_cvt_pk_bf16_f32 v63, v123, v127\n\t"
    "s_mov_b64 exec, -1\n\t"
    "s_waitcnt lgkmcnt(0)\n\t"
    "s_nop 1\n\t"
    "v_mfma_f32_16x16x32_bf16 v[64:67], a[64:67], v[60:63], v[64:67]\n\t"
    "v_mfma_f32_16x16x32_bf16 v[68:71], a[68:71], v[60:63], v[68:71]\n\t"
    "v_mfma_f32_16x16x32_bf16 v[72:75], a[72:75], v[60:63], v[72:75]\n\t"
    "v_mfma_f32_16x16x32_bf16 v[76:79], a[76:79], v[60:63], v[76:79]\n\t"
    "buffer_load_dwordx4 v[160:163], %[vo0], %[rkv], %[so1] offen nt\n\t"
    "buffer_load_dwordx4 v[164:167], %[vo0], %[rkv], %[so1] offen offset:256 nt\n\t"
    "buffer_load_dwordx4 a[16:19], %[ef0], %[re], %[se1] offen\n\t"
    "buffer_load_dwordx4 v[168:171], %[vo0], %[rkv], %[so1] offen offset:512 nt\n\t"
    "buffer_load_dwordx4 v[172:175], %[vo0], %[rkv], %[so1] offen offset:768 nt\n\t"
    "buffer_load_dwordx4 a[20:23], %[ef1], %[re], %[se1] offen\n\t"
    "buffer_load_dwordx4 v[176:179], %[vo1], %[rkv], %[so1] offen nt\n\t"
    "buffer_load_dwordx4 v[180:183], %[vo1], %[rkv], %[so1] offen offset:256 nt\n\t"
    "buffer_load_dwordx4 a[24:27], %[ef2], %[re], %[se1] offen\n\t"
    "buffer_load_dwordx4 v[184:187], %[vo1], %[rkv], %[so1] offen offset:512 nt\n\t"
    "buffer_load_dwordx4 v[188:191], %[vo1], %[rkv], %[so1] offen offset:768 nt\n\t"
    "buffer_load_dwordx4 a[28:31], %[ef3], %[re], %[se1] offen\n\t"
    "s_waitcnt vmcnt(36)\n\t"
    "ds_write_b128 %[ewr0], a[32:35] offset:4096\n\t"
    "ds_write_b128 %[ewr1], a[36:39] offset:5120\n\t"
    "ds_write_b128 %[ewr0], a[40:43] offset:6144\n\t"
    "ds_write_b128 %[ewr1], a[44:47] offset:7168\n\t"
    "ds_read_b128 a[64:67], %[erd0]\n\t"
    "ds_read_b128 a[68:71], %[erd0] offset:2048\n\t"
    "ds_read_b128 a[72:75], %[erd0] offset:4096\n\t"
    "ds_read_b128 a[76:79], %[erd0] offset:6144\n\t"
    "v_mfma_f32_16x16x32_bf16 v[96:99], v[192:195], v[48:51], 0\n\t"
    "v_mfma_f32_16x16x32_bf16 v[100:103], v[196:199], v[48:51], 0\n\t"
    "v_mfma_f32_16x16x32_bf16 v[104:107], v[200:203], v[48:51], 0\n\t"
    "v_mfma_f32_16x16x32_bf16 v[108:111], v[204:207], v[48:51], 0\n\t"
    "v_mfma_f32_16x16x32_bf16 v[112:115], v[208:211], v[48:51], 0\n\t"
    "v_mfma_f32_16x16x32_bf16 v[116:119], v[212:215], v[48:51], 0\n\t"
    "v_mfma_f32_16x16x32_bf16 v[120:123], v[216:219], v[48:51], 0\n\t"
    "v_mfma_f32_16x16x32_bf16 v[124:127], v[220:223], v[48:51], 0\n\t"
    "s_nop 4\n\t"
    "s_mov_b32 exec_lo, 0x000f000f\n\t"
    "s_mov_b32 exec_hi, 0x000f000f\n\t"
    "v_cvt_pk_bf16_f32 v60, v96, v100\n\t"
    "v_cvt_pk_bf16_f32 v61, v104, v108\n\t"
    "v_cvt_pk_bf16_f32 v62, v112, v116\n\t"
    "v_cvt_pk_bf16_f32 v63, v120, v124\n\t"
    "s_mov_b32 exec_lo, 0x00f000f0\n\t"
    "s_mov_b32 exec_hi, 0x00f000f0\n\t"
    "v_cvt_pk_bf16_f32 v60, v97, v101\n\t"
    "v_cvt_pk_bf16_f32 v61, v105, v109\n\t"
    "v_cvt_pk_bf16_f32 v62, v113, v117\n\t"
    "v_cvt_pk_bf16_f32 v63, v121, v125\n\t"
    "s_mov_b32 exec_lo, 0x0f000f00\n\t"
    "s_mov_b32 exec_hi, 0x0f000f00\n\t"
    "v_cvt_pk_bf16_f32 v60, v98, v102\n\t"
    "v_cvt_pk_bf16_f32 v61, v106, v110\n\t"
    "v_cvt_pk_bf16_f32 v62, v114, v118\n\t"
    "v_cvt_pk_bf16_f32 v63, v122, v126\n\t"
    "s_mov_b32 exec_lo, 0xf000f000\n\t"
    "s_mov_b32 exec_hi, 0xf000f000\n\t"
    "v_cvt_pk_bf16_f32 v60, v99, v103\n\t"
    "v_cvt_pk_bf16_f32 v61, v107, v111\n\t"
    "v_cvt_pk_bf16_f32 v62, v115, v119\n\t"
    "v_cvt_pk_bf16_f32 v63, v123, v127\n\t"
    "s_mov_b64 exec, -1\n\t"
    "s_waitcnt lgkmcnt(0)\n\t"
    "s_nop 1\n\t"
    "v_mfma_f32_16x16x32_bf16 v[64:67], a[64:67], v[60:63], v[64:67]\n\t"
    "v_mfma_f32_16x16x32_bf16 v[68:71], a[68:71], v[60:63], v[68:71]\n\t"
    "v_mfma_f32_16x16x32_bf16 v[72:75], a[72:75], v[60:63], v[72:75]\n\t"
    "v_mfma_f32_16x16x32_bf16 v[76:79], a[76:79], v[60:63], v[76:79]\n\t"
    "buffer_load_dwordx4 v[192:195], %[vo0], %[rkv], %[so2] offen nt\n\t"
    "buffer_load_dwordx4 v[196:199], %[vo0], %[rkv], %[so2] offen offset:256 nt\n\t"
    "buffer_load_dwordx4 a[32:35], %[ef0], %[re], %[se2] offen\n\t"
    "buffer_load_dwordx4 v[200:203], %[vo0], %[rkv], %[so2] offen offset:512 nt\n\t"
    "buffer_load_dwordx4 v[204:207], %[vo0], %[rkv], %[so2] offen offset:768 nt\n\t"
    "buffer_load_dwordx4 a[36:39], %[ef1], %[re], %[se2] offen\n\t"
    "buffer_load_dwordx4 v[208:211], %[vo1], %[rkv], %[so2] offen nt\n\t"
    "buffer_load_dwordx4 v[212:215], %[vo1], %[rkv], %[so2] offen offset:256 nt\n\t"
    "buffer_load_dwordx4 a[40:43], %[ef2], %[re], %[se2] offen\n\t"
    "buffer_load_dwordx4 v[216:219], %[vo1], %[rkv], %[so2] offen offset:512 nt\n\t"
    "buffer_load_dwordx4 v[220:223], %[vo1], %[rkv], %[so2] offen offset:768 nt\n\t"
    "buffer_load_dwordx4 a[44:47], %[ef3], %[re], %[se2] offen\n\t"
    "s_waitcnt vmcnt(36)\n\t"
    "ds_read_b128 a[64:67], %[erd1]\n\t"
    "ds_read_b128 a[68:71], %[erd1] offset:2048\n\t"
    "ds_read_b128 a[72:75], %[erd1] offset:4096\n\t"
    "ds_read_b128 a[76:79], %[erd1] offset:6144\n\t"
    "ds_write_b128 %[ewr0], a[48:51]\n\t"
    "ds_write_b128 %[ewr1], a[52:55] offset:1024\n\t"
    "ds_write_b128 %[ewr0], a[56:59] offset:2048\n\t"
    "ds_write_b128 %[ewr1], a[60:63] offset:3072\n\t"
    "v_mfma_f32_16x16x32_bf16 v[96:99], v[224:227], v[48:51], 0\n\t"
    "v_mfma_f32_16x16x32_bf16 v[100:103], v[228:231], v[48:51], 0\n\t"
    "v_mfma_f32_16x16x32_bf16 v[104:107], v[232:235], v[48:51], 0\n\t"
    "v_mfma_f32_16x16x32_bf16 v[108:111], v[236:239], v[48:51], 0\n\t"
    "v_mfma_f32_16x16x32_bf16 v[112:115], v[240:243], v[48:51], 0\n\t"
    "v_mfma_f32_16x16x32_bf16 v[116:119], v[244:247], v[48:51], 0\n\t"
    "v_mfma_f32_16x16x32_bf16 v[120:123], v[248:251], v[48:51], 0\n\t"
    "v_mfma_f32_16x16x32_bf16 v[124:127], v[252:255], v[48:51], 0\n\t"
    "s_nop 4\n\t"
    "s_mov_b32 exec_lo, 0x000f000f\n\t"
    "s_mov_b32 exec_hi, 0x000f000f\n\t"
    "v_cvt_pk_bf16_f32 v60, v96, v100\n\t"
    "v_cvt_pk_bf16_f32 v61, v104, v108\n\t"
    "v_cvt_pk_bf16_f32 v62, v112, v116\n\t"
    "v_cvt_pk_bf16_f32 v63, v120, v124\n\t"
    "s_mov_b32 exec_lo, 0x00f000f0\n\t"
    "s_mov_b32 exec_hi, 0x00f000f0\n\t"
    "v_cvt_pk_bf16_f32 v60, v97, v101\n\t"
    "v_cvt_pk_bf16_f32 v61, v105, v109\n\t"
    "v_cvt_pk_bf16_f32 v62, v113, v117\n\t"
    "v_cvt_pk_bf16_f32 v63, v121, v125\n\t"
    "s_mov_b32 exec_lo, 0x0f000f00\n\t"
    "s_mov_b32 exec_hi, 0x0f000f00\n\t"
    "v_cvt_pk_bf16_f32 v60, v98, v102\n\t"
    "v_cvt_pk_bf16_f32 v61, v106, v110\n\t"
    "v_cvt_pk_bf16_f32 v62, v114, v118\n\t"
    "v_cvt_pk_bf16_f32 v63, v122, v126\n\t"
    "s_mov_b32 exec_lo, 0xf000f000\n\t"
    "s_mov_b32 exec_hi, 0xf000f000\n\t"
    "v_cvt_pk_bf16_f32 v60, v99, v103\n\t"
    "v_cvt_pk_bf16_f32 v61, v107, v111\n\t"
    "v_cvt_pk_bf16_f32 v62, v115, v119\n\t"
    "v_cvt_pk_bf16_f32 v63, v123, v127\n\t"
    "s_mov_b64 exec, -1\n\t"
    "s_waitcnt lgkmcnt(0)\n\t"
    "s_nop 1\n\t"
    "v_mfma_f32_16x16x32_bf16 v[64:67], a[64:67], v[60:63], v[64:67]\n\t"
    "v_mfma_f32_16x16x32_bf16 v[68:71], a[68:71], v[60:63], v[68:71]\n\t"
    "v_mfma_f32_16x16x32_bf16 v[72:75], a[72:75], v[60:63], v[72:75]\n\t"
    "v_mfma_f32_16x16x32_bf16 v[76:79], a[76:79], v[60:63], v[76:79]\n\t"
    "s_nop 7\n\t"
    "v_add_f32_dpp v64, v64, v64 row_ror:4 row_mask:0xf bank_mask:0xf\n\t"
    "v_add_f32_dpp v65, v65, v65 row_ror:4 row_mask:0xf bank_mask:0xf\n\t"
    "v_add_f32_dpp v66, v66, v66 row_ror:4 row_mask:0xf bank_mask:0xf\n\t"
    "v_add_f32_dpp v67, v67, v67 row_ror:4 row_mask:0xf bank_mask:0xf\n\t"
    "v_add_f32_dpp v68, v68, v68 row_ror:4 row_mask:0xf bank_mask:0xf\n\t"
    "v_add_f32_dpp v69, v69, v69 row_ror:4 row_mask:0xf bank_mask:0xf\n\t"
    "v_add_f32_dpp v70, v70, v70 row_ror:4 row_mask:0xf bank_mask:0xf\n\t"
    "v_add_f32_dpp v71, v71, v71 row_ror:4 row_mask:0xf bank_mask:0xf\n\t"
    "v_add_f32_dpp v72, v72, v72 row_ror:4 row_mask:0xf bank_mask:0xf\n\t"
    "v_add_f32_dpp v73, v73, v73 row_ror:4 row_mask:0xf bank_mask:0xf\n\t"
    "v_add_f32_dpp v74, v74, v74 row_ror:4 row_mask:0xf bank_mask:0xf\n\t"
    "v_add_f32_dpp v75, v75, v75 row_ror:4 row_mask:0xf bank_mask:0xf\n\t"
    "v_add_f32_dpp v76, v76, v76 row_ror:4 row_mask:0xf bank_mask:0xf\n\t"
    "v_add_f32_dpp v77, v77, v77 row_ror:4 row_mask:0xf bank_mask:0xf\n\t"
    "v_add_f32_dpp v78, v78, v78 row_ror:4 row_mask:0xf bank_mask:0xf\n\t"
    "v_add_f32_dpp v79, v79, v79 row_ror:4 row_mask:0xf bank_mask:0xf\n\t"
    "v_add_f32_dpp v64, v64, v64 row_ror:8 row_mask:0xf bank_mask:0xf\n\t"
    "v_add_f32_dpp v65, v65, v65 row_ror:8 row_mask:0xf bank_mask:0xf\n\t"
    "v_add_f32_dpp v66, v66, v66 row_ror:8 row_mask:0xf bank_mask:0xf\n\t"
    "v_add_f32_dpp v67, v67, v67 row_ror:8 row_mask:0xf bank_mask:0xf\n\t"
    "v_add_f32_dpp v68, v68, v68 row_ror:8 row_mask:0xf bank_mask:0xf\n\t"
    "v_add_f32_dpp v69, v69, v69 row_ror:8 row_mask:0xf bank_mask:0xf\n\t"
    "v_add_f32_dpp v70, v70, v70 row_ror:8 row_mask:0xf bank_mask:0xf\n\t"
    "v_add_f32_dpp v71, v71, v71 row_ror:8 row_mask:0xf bank_mask:0xf\n\t"
    "v_add_f32_dpp v72, v72, v72 row_ror:8 row_mask:0xf bank_mask:0xf\n\t"
    "v_add_f32_dpp v73, v73, v73 row_ror:8 row_mask:0xf bank_mask:0xf\n\t"
    "v_add_f32_dpp v74, v74, v74 row_ror:8 row_mask:0xf bank_mask:0xf\n\t"
    "v_add_f32_dpp v75, v75, v75 row_ror:8 row_mask:0xf bank_mask:0xf\n\t"
    "v_add_f32_dpp v76, v76, v76 row_ror:8 row_mask:0xf bank_mask:0xf\n\t"
    "v_add_f32_dpp v77, v77, v77 row_ror:8 row_mask:0xf bank_mask:0xf\n\t"
    "v_add_f32_dpp v78, v78, v78 row_ror:8 row_mask:0xf bank_mask:0xf\n\t"
    "v_add_f32_dpp v79, v79, v79 row_ror:8 row_mask:0xf bank_mask:0xf\n\t"
    "s_mov_b32 exec_lo, 0x000f000f\n\t"
    "s_mov_b32 exec_hi, 0x000f000f\n\t"
    "ds_write_b128 %[pwr], v[64:67]\n\t"
    "ds_write_b128 %[pwr], v[68:71] offset:64\n\t"
    "ds_write_b128 %[pwr], v[72:75] offset:128\n\t"
    "ds_write_b128 %[pwr], v[76:79] offset:192\n\t"
    "s_mov_b64 exec, -1\n\t"
    "s_waitcnt lgkmcnt(0)\n\t"
    "s_barrier\n\t"
    "ds_read_b128 v[96:99], %[prd]\n\t"
    "ds_read_b128 v[100:103], %[prd] offset:1024\n\t"
    "v_mov_b32 v64, 0\n\t"
    "v_mov_b32 v65, 0\n\t"
    "v_mov_b32 v66, 0\n\t"
    "v_mov_b32 v67, 0\n\t"
    "v_mov_b32 v68, 0\n\t"
    "v_mov_b32 v69, 0\n\t"
    "v_mov_b32 v70, 0\n\t"
    "v_mov_b32 v71, 0\n\t"
    "v_mov_b32 v72, 0\n\t"
    "v_mov_b32 v73, 0\n\t"
    "v_mov_b32 v74, 0\n\t"
    "v_mov_b32 v75, 0\n\t"
    "v_mov_b32 v76, 0\n\t"
    "v_mov_b32 v77, 0\n\t"
    "v_mov_b32 v78, 0\n\t"
    "v_mov_b32 v79, 0\n\t"
    "v_mov_b32 v80, 0\n\t"
    "v_mov_b32 v81, 0\n\t"
    "v_mov_b32 v82, 0\n\t"
    "v_mov_b32 v83, 0\n\t"
    "v_mov_b32 v84, 0\n\t"
    "v_mov_b32 v85, 0\n\t"
    "v_mov_b32 v86, 0\n\t"
    "v_mov_b32 v87, 0\n\t"
    "v_mov_b32 v88, 0\n\t"
    "v_mov_b32 v89, 0\n\t"
    "v_mov_b32 v90, 0\n\t"
    "v_mov_b32 v91, 0\n\t"
    "v_mov_b32 v92, 0\n\t"
    "v_mov_b32 v93, 0\n\t"
    "v_mov_b32 v94, 0\n\t"
    "v_mov_b32 v95, 0\n\t"
    "s_waitcnt lgkmcnt(0)\n\t"
    "v_add_f32 v96, v96, v100\n\t"
    "v_add_f32 v97, v97, v101\n\t"
    "v_add_f32 v98, v98, v102\n\t"
    "v_add_f32 v99, v99, v103\n\t"
    "v_max3_f32 v100, v96, v97, v98\n\t"
    "v_max_f32 v100, v100, v99\n\t"
    "s_nop 1\n\t"
    "v_max_f32_dpp v100, v100, v100 quad_perm:[1,0,3,2] row_mask:0xf bank_mask:0xf\n\t"
    "s_nop 1\n\t"
    "v_max_f32_dpp v100, v100, v100 quad_perm:[2,3,0,1] row_mask:0xf bank_mask:0xf\n\t"
    "v_mov_b32 v101, v100\n\t"
    "s_nop 1\n\t"
    "v_permlane16_swap_b32 v100, v101\n\t"
    "s_nop 1\n\t"
    "v_max_f32 v100, v100, v101\n\t"
    "v_mov_b32 v101, v100\n\t"
    "s_nop 1\n\t"
    "v_permlane32_swap_b32 v100, v101\n\t"
    "s_nop 1\n\t"
    "v_max_f32 v100, v100, v101\n\t"
    "v_sub_f32 v96, v96, v100\n\t"
    "v_sub_f32 v97, v97, v100\n\t"
    "v_sub_f32 v98, v98, v100\n\t"
    "v_sub_f32 v99, v99, v100\n\t"
    "v_mul_f32 v96, %[cexp], v96\n\t"
    "v_mul_f32 v97, %[cexp], v97\n\t"
    "v_mul_f32 v98, %[cexp], v98\n\t"
    "v_mul_f32 v99, %[cexp], v99\n\t"
    "v_exp_f32 v96, v96\n\t"
    "v_exp_f32 v97, v97\n\t"
    "v_exp_f32 v98, v98\n\t"
    "v_exp_f32 v99, v99\n\t"
    "s_nop 0\n\t"
    "v_add_f32 v100, v96, v97\n\t"
    "v_add_f32 v101, v98, v99\n\t"
    "v_add_f32 v100, v100, v101\n\t"
    "s_nop 1\n\t"
    "v_add_f32_dpp v100, v100, v100 quad_perm:[1,0,3,2] row_mask:0xf bank_mask:0xf\n\t"
    "s_nop 1\n\t"
    "v_add_f32_dpp v100, v100, v100 quad_perm:[2,3,0,1] row_mask:0xf bank_mask:0xf\n\t"
    "v_mov_b32 v101, v100\n\t"
    "s_nop 1\n\t"
    "v_permlane16_swap_b32 v100, v101\n\t"
    "s_nop 1\n\t"
    "v_add_f32 v100, v100, v101\n\t"
    "v_mov_b32 v101, v100\n\t"
    "s_nop 1\n\t"
    "v_permlane32_swap_b32 v100, v101\n\t"
    "s_nop 1\n\t"
    "v_add_f32 v100, v100, v101\n\t"
    "v_rcp_f32 v100, v100\n\t"
    "s_nop 0\n\t"
    "v_mul_f32 v96, v96, v100\n\t"
    "v_mul_f32 v97, v97, v100\n\t"
    "v_mul_f32 v98, v98, v100\n\t"
    "v_mul_f32 v99, v99, v100\n\t"
    "v_cvt_pk_bf16_f32 v102, v96, v97\n\t"
    "v_cvt_pk_bf16_f32 v103, v98, v99\n\t"
    "s_nop 1\n\t"
    "v_mov_b32_dpp v40, v102 quad_perm:[0,0,0,0] row_mask:0xf bank_mask:0xf\n\t"
    "v_mov_b32_dpp v41, v103 quad_perm:[0,0,0,0] row_mask:0xf bank_mask:0xf\n\t"
    "v_mov_b32_dpp v42, v102 quad_perm:[1,1,1,1] row_mask:0xf bank_mask:0xf\n\t"
    "v_mov_b32_dpp v43, v103 quad_perm:[1,1,1,1] row_mask:0xf bank_mask:0xf\n\t"
    "v_mov_b32_dpp v44, v102 quad_perm:[2,2,2,2] row_mask:0xf bank_mask:0xf\n\t"
    "v_mov_b32_dpp v45, v103 quad_perm:[2,2,2,2] row_mask:0xf bank_mask:0xf\n\t"
    "v_mov_b32_dpp v46, v102 quad_perm:[3,3,3,3] row_mask:0xf bank_mask:0xf\n\t"
    "v_mov_b32_dpp v47, v103 quad_perm:[3,3,3,3] row_mask:0xf bank_mask:0xf\n\t"
    "buffer_load_dwordx4 v[224:227], %[vo0], %[rkv], %[so3] offen nt\n\t"
    "buffer_load_dwordx4 v[228:231], %[vo0], %[rkv], %[so3] offen offset:256 nt\n\t"
    "buffer_load_dwordx4 a[48:51], %[ef0], %[re4], %[se3] offen\n\t"
    "buffer_load_dwordx4 v[232:235], %[vo0], %[rkv], %[so3] offen offset:512 nt\n\t"
    "buffer_load_dwordx4 v[236:239], %[vo0], %[rkv], %[so3] offen offset:768 nt\n\t"
    "buffer_load_dwordx4 a[52:55], %[ef1], %[re4], %[se3] offen\n\t"
    "buffer_load_dwordx4 v[240:243], %[vo1], %[rkv], %[so3] offen nt\n\t"
    "buffer_load_dwordx4 v[244:247], %[vo1], %[rkv], %[so3] offen offset:256 nt\n\t"
    "buffer_load_dwordx4 a[56:59], %[ef2], %[re4], %[se3] offen\n\t"
    "buffer_load_dwordx4 v[248:251], %[vo1], %[rkv], %[so3] offen offset:512 nt\n\t"
    "buffer_load_dwordx4 v[252:255], %[vo1], %[rkv], %[so3] offen offset:768 nt\n\t"
    "buffer_load_dwordx4 a[60:63], %[ef3], %[re4], %[se3] offen\n\t"
          : RING_OPS, "+{v[64:79]}"(sacc), "=&{v[80:95]}"(ohi), "=&{v[40:47]}"(pb)
          : LOAD_OPS, "{v[48:51]}"(qb), [ewr0] "v"(ewr0), [ewr1] "v"(ewr1), [erd0] "v"(erd0), [erd1] "v"(erd1),
            [pwr] "v"(pwr), [prd] "v"(prd), [cexp] "s"(0.08838834764831845f * 1.4426950408889634f)
          : "memory", T_CLOB);
      oacc = __builtin_shufflevector(sacc, ohi, 0, 1, 2, 3, 4, 5, 6, 7, 8, 9, 10, 11, 12, 13, 14, 15, 16, 17, 18, 19, 20,
                                     21, 22, 23, 24, 25, 26, 27, 28, 29, 30, 31);
    }
  }

  const unsigned trd0 = tile + (8 * g + (c >> 2)) * 128 + ((((c >> 1) & 1) | ((c >> 2) & 2) | ((g & 1) << 2)) << 4) + (c & 1) * 8;
  const unsigned trd1 = trd0 ^ 32, trd2 = trd0 ^ 64, trd3 = trd0 ^ 96;
#define V_OPS                                                                                                 \
  "{v[40:47]}"(pb), [ewr0] "v"(ewr0), [ewr1] "v"(ewr1), [trd0] "v"(trd0), [trd1] "v"(trd1), [trd2] "v"(trd2), \
  [trd3] "v"(trd3), [sel0] "s"(0x05040100u), [sel1] "s"(0x07060302u)
#pragma nounroll
  for (int i = 0; i < NB - 2; i += 2) {
    const u32x4 rkv = mkdesc(vh, SKV * DH * 2);
    const u32x4 re = mkdesc(f_, RR * SKV * 2), re4 = mkdesc(f_, i + 4 < NB ? RR * SKV * 2 : 0);
    const int bA = BLK(i + 2), bB = BLK(i + 3), bC = BLK(i + 4);
    const unsigned vo0 = voV, vo1 = voV + 4096;
    asm volatile(
    "s_waitcnt vmcnt(36)\n\t"
    "ds_write_b128 %[ewr0], a[0:3] offset:4096\n\t"
    "ds_write_b128 %[ewr1], a[4:7] offset:5120\n\t"
    "ds_write_b128 %[ewr0], a[8:11] offset:6144\n\t"
    "ds_write_b128 %[ewr1], a[12:15] offset:7168\n\t"
    "ds_read_b64_tr_b16 a[64:65], %[trd0]\n\t"
    "ds_read_b64_tr_b16 a[66:67], %[trd0] offset:512\n\t"
    "ds_read_b64_tr_b16 a[68:69], %[trd0] offset:4096\n\t"
    "ds_read_b64_tr_b16 a[70:71], %[trd0] offset:4608\n\t"
    "ds_read_b64_tr_b16 a[72:73], %[trd1]\n\t"
    "ds_read_b64_tr_b16 a[74:75], %[trd1] offset:512\n\t"
    "ds_read_b64_tr_b16 a[76:77], %[trd1] offset:4096\n\t"
    "ds_read_b64_tr_b16 a[78:79], %[trd1] offset:4608\n\t"
    "v_perm_b32 v96, v132, v128, %[sel0]\n\t"
    "v_perm_b32 v97, v140, v136, %[sel0]\n\t"
    "v_perm_b32 v98, v148, v144, %[sel0]\n\t"
    "v_perm_b32 v99, v156, v152, %[sel0]\n\t"
    "v_perm_b32 v100, v132, v128, %[sel1]\n\t"
    "v_perm_b32 v101, v140, v136, %[sel1]\n\t"
    "v_perm_b32 v102, v148, v144, %[sel1]\n\t"
    "v_perm_b32 v103, v156, v152, %[sel1]\n\t"
    "v_perm_b32 v104, v133, v129, %[sel0]\n\t"
    "v_perm_b32 v105, v141, v137, %[sel0]\n\t"
    "v_perm_b32 v106, v149, v145, %[sel0]\n\t"
    "v_perm_b32 v107, v157, v153, %[sel0]\n\t"
    "v_perm_b32 v108, v133, v129, %[sel1]\n\t"
    "v_perm_b32 v109, v141, v137, %[sel1]\n\t"
    "v_perm_b32 v110, v149, v145, %[sel1]\n\t"
    "v_perm_b32 v111, v157, v153, %[sel1]\n\t"
    "s_waitcnt lgkmcnt(0)\n\t"
    "v_mfma_f32_16x16x32_bf16 v[52:55], a[64:67], v[40:43], 0\n\t"
    "v_mfma_f32_16x16x32_bf16 v[56:59], a[72:75], v[40:43], 0\n\t"
    "v_mfma_f32_16x16x32_bf16 v[52:55], a[68:71], v[44:47], v[52:55]\n\t"
    "v_mfma_f32_16x16x32_bf16 v[56:59], a[76:79], v[44:47], v[56:59]\n\t"
    "v_perm_b32 v112, v134, v130, %[sel0]\n\t"
    "v_perm_b32 v113, v142, v138, %[sel0]\n\t"
    "v_perm_b32 v114, v150, v146, %[sel0]\n\t"
    "v_perm_b32 v115, v158, v154, %[sel0]\n\t"
    "v_perm_b32 v116, v134, v130, %[sel1]\n\t"
    "v_perm_b32 v117, v142, v138, %[sel1]\n\t"
    "v_perm_b32 v118, v150, v146, %[sel1]\n\t"
    "v_perm_b32 v119, v158, v154, %[sel1]\n\t"
    "v_perm_b32 v120, v135, v131, %[sel0]\n\t"
    "v_perm_b32 v121, v143, v139, %[sel0]\n\t"
    "v_perm_b32 v122, v151, v147, %[sel0]\n\t"
    "v_perm_b32 v123, v159, v155, %[sel0]\n\t"
    "v_perm_b32 v124, v135, v131, %[sel1]\n\t"
    "v_perm_b32 v125, v143, v139, %[sel1]\n\t"
    "v_perm_b32 v126, v151, v147, %[sel1]\n\t"
    "v_perm_b32 v127, v159, v155, %[sel1]\n\t"
    "v_cvt_pk_bf16_f32 v60, v52, v53\n\t"
    "v_cvt_pk_bf16_f32 v61, v54, v55\n\t"
    "v_cvt_pk_bf16_f32 v62, v56, v57\n\t"
    "v_cvt_pk_bf16_f32 v63, v58, v59\n\t"
    "s_nop 1\n\t"
    "v_mfma_f32_16x16x32_bf16 v[64:67], v[60:63], v[96:99], v[64:67]\n\t"
    "v_mfma_f32_16x16x32_bf16 v[68:71], v[60:63], v[100:103], v[68:71]\n\t"
    "v_mfma_f32_16x16x32_bf16 v[72:75], v[60:63], v[104:107], v[72:75]\n\t"
    "v_mfma_f32_16x16x32_bf16 v[76:79], v[60:63], v[108:111], v[76:79]\n\t"
    "v_mfma_f32_16x16x32_bf16 v[80:83], v[60:63], v[112:115], v[80:83]\n\t"
    "v_mfma_f32_16x16x32_bf16 v[84:87], v[60:63], v[116:119], v[84:87]\n\t"
    "v_mfma_f32_16x16x32_bf16 v[88:91], v[60:63], v[120:123], v[88:91]\n\t"
    "v_mfma_f32_16x16x32_bf16 v[92:95], v[60:63], v[124:127], v[92:95]\n\t"
    "buffer_load_dwordx4 v[128:131], %[vo0], %[rkv], %[so0] offen nt\n\t"
    "buffer_load_dwordx4 v[132:135], %[vo0], %[rkv], %[so0] offen offset:256 nt\n\t"
    "buffer_load_dwordx4 a[0:3], %[ef0], %[re], %[se0] offen\n\t"
    "buffer_load_dwordx4 v[136:139], %[vo0], %[rkv], %[so0] offen offset:512 nt\n\t"
    "buffer_load_dwordx4 v[140:143], %[vo0], %[rkv], %[so0] offen offset:768 nt\n\t"
    "buffer_load_dwordx4 a[4:7], %[ef1], %[re], %[se0] offen\n\t"
    "buffer_load_dwordx4 v[144:147], %[vo1], %[rkv], %[so0] offen nt\n\t"
    "buffer_load_dwordx4 v[148:151], %[vo1], %[rkv], %[so0] offen offset:256 nt\n\t"
    "buffer_load_dwordx4 a[8:11], %[ef2], %[re], %[se0] offen\n\t"
    "buffer_load_dwordx4 v[152:155], %[vo1], %[rkv], %[so0] offen offset:512 nt\n\t"
    "buffer_load_dwordx4 v[156:159], %[vo1], %[rkv], %[so0] offen offset:768 nt\n\t"
    "buffer_load_dwordx4 a[12:15], %[ef3], %[re], %[se0] offen\n\t"
    "ds_read_b64_tr_b16 a[64:65], %[trd2]\n\t"
    "ds_read_b64_tr_b16 a[66:67], %[trd2] offset:512\n\t"
    "ds_read_b64_tr_b16 a[68:69], %[trd2] offset:4096\n\t"
    "ds_read_b64_tr_b16 a[70:71], %[trd2] offset:4608\n\t"
    "ds_read_b64_tr_b16 a[72:73], %[trd3]\n\t"
    "ds_read_b64_tr_b16 a[74:75], %[trd3] offset:512\n\t"
    "ds_read_b64_tr_b16 a[76:77], %[trd3] offset:4096\n\t"
    "ds_read_b64_tr_b16 a[78:79], %[trd3] offset:4608\n\t"
    "s_waitcnt lgkmcnt(0)\n\t"
    "v_mfma_f32_16x16x32_bf16 v[52:55], a[64:67], v[40:43], 0\n\t"
    "v_mfma_f32_16x16x32_bf16 v[56:59], a[72:75], v[40:43], 0\n\t"
    "v_mfma_f32_16x16x32_bf16 v[52:55], a[68:71], v[44:47], v[52:55]\n\t"
    "v_mfma_f32_16x16x32_bf16 v[56:59], a[76:79], v[44:47], v[56:59]\n\t"
    "s_nop 7\n\t"
    "v_cvt_pk_bf16_f32 v60, v52, v53\n\t"
    "v_cvt_pk_bf16_f32 v61, v54, v55\n\t"
    "v_cvt_pk_bf16_f32 v62, v56, v57\n\t"
    "v_cvt_pk_bf16_f32 v63, v58, v59\n\t"
    "s_waitcnt vmcnt(36)\n\t"
    "ds_write_b128 %[ewr0], a[16:19]\n\t"
    "ds_write_b128 %[ewr1], a[20:23] offset:1024\n\t"
    "ds_write_b128 %[ewr0], a[24:27] offset:2048\n\t"
    "ds_write_b128 %[ewr1], a[28:31] offset:3072\n\t"
    "v_perm_b32 v96, v164, v160, %[sel0]\n\t"
    "v_perm_b32 v97, v172, v168, %[sel0]\n\t"
    "v_perm_b32 v98, v180, v176, %[sel0]\n\t"
    "v_perm_b32 v99, v188, v184, %[sel0]\n\t"
    "v_perm_b32 v100, v164, v160, %[sel1]\n\t"
    "v_perm_b32 v101, v172, v168, %[sel1]\n\t"
    "v_perm_b32 v102, v180, v176, %[sel1]\n\t"
    "v_perm_b32 v103, v188, v184, %[sel1]\n\t"
    "v_perm_b32 v104, v165, v161, %[sel0]\n\t"
    "v_perm_b32 v105, v173, v169, %[sel0]\n\t"
    "v_perm_b32 v106, v181, v177, %[sel0]\n\t"
    "v_perm_b32 v107, v189, v185, %[sel0]\n\t"
    "v_perm_b32 v108, v165, v161, %[sel1]\n\t"
    "v_perm_b32 v109, v173, v169, %[sel1]\n\t"
    "v_perm_b32 v110, v181, v177, %[sel1]\n\t"
    "v_perm_b32 v111, v189, v185, %[sel1]\n\t"
    "v_mfma_f32_16x16x32_bf16 v[64:67], v[60:63], v[96:99], v[64:67]\n\t"
    "v_mfma_f32_16x16x32_bf16 v[68:71], v[60:63], v[100:103], v[68:71]\n\t"
    "v_perm_b32 v112, v166, v162, %[sel0]\n\t"
    "v_perm_b32 v113, v174, v170, %[sel0]\n\t"
    "v_perm_b32 v114, v182, v178, %[sel0]\n\t"
    "v_perm_b32 v115, v190, v186, %[sel0]\n\t"
    "v_mfma_f32_16x16x32_bf16 v[72:75], v[60:63], v[104:107], v[72:75]\n\t"
    "v_perm_b32 v116, v166, v162, %[sel1]\n\t"
    "v_perm_b32 v117, v174, v170, %[sel1]\n\t"
    "v_perm_b32 v118, v182, v178, %[sel1]\n\t"
    "v_perm_b32 v119, v190, v186, %[sel1]\n\t"
    "v_mfma_f32_16x16x32_bf16 v[76:79], v[60:63], v[108:111], v[76:79]\n\t"
    "v_perm_b32 v120, v167, v163, %[sel0]\n\t"
    "v_perm_b32 v121, v175, v171, %[sel0]\n\t"
    "v_perm_b32 v122, v183, v179, %[sel0]\n\t"
    "v_perm_b32 v123, v191, v187, %[sel0]\n\t"
    "v_mfma_f32_16x16x32_bf16 v[80:83], v[60:63], v[112:115], v[80:83]\n\t"
    "v_perm_b32 v124, v167, v163, %[sel1]\n\t"
    "v_perm_b32 v125, v175, v171, %[sel1]\n\t"
    "v_perm_b32 v126, v183, v179, %[sel1]\n\t"
    "v_perm_b32 v127, v191, v187, %[sel1]\n\t"
    "v_mfma_f32_16x16x32_bf16 v[84:87], v[60:63], v[116:119], v[84:87]\n\t"
    "v_mfma_f32_16x16x32_bf16 v[88:91], v[60:63], v[120:123], v[88:91]\n\t"
    "v_mfma_f32_16x16x32_bf16 v[92:95], v[60:63], v[124:127], v[92:95]\n\t"
    "s_waitcnt lgkmcnt(0)\n\t"
    "buffer_load_dwordx4 v[160:163], %[vo0], %[rkv], %[so1] offen nt\n\t"
    "buffer_load_dwordx4 v[164:167], %[vo0], %[rkv], %[so1] offen offset:256 nt\n\t"
    "buffer_load_dwordx4 a[16:19], %[ef0], %[re], %[se1] offen\n\t"
    "buffer_load_dwordx4 v[168:171], %[vo0], %[rkv], %[so1] offen offset:512 nt\n\t"
    "buffer_load_dwordx4 v[172:175], %[vo0], %[rkv], %[so1] offen offset:768 nt\n\t"
    "buffer_load_dwordx4 a[20:23], %[ef1], %[re], %[se1] offen\n\t"
    "buffer_load_dwordx4 v[176:179], %[vo1], %[rkv], %[so1] offen nt\n\t"
    "buffer_load_dwordx4 v[180:183], %[vo1], %[rkv], %[so1] offen offset:256 nt\n\t"
    "buffer_load_dwordx4 a[24:27], %[ef2], %[re], %[se1] offen\n\t"
    "buffer_load_dwordx4 v[184:187], %[vo1], %[rkv], %[so1] offen offset:512 nt\n\t"
    "buffer_load_dwordx4 v[188:191], %[vo1], %[rkv], %[so1] offen offset:768 nt\n\t"
    "buffer_load_dwordx4 a[28:31], %[ef3], %[re], %[se1] offen\n\t"
    "s_waitcnt vmcnt(36)\n\t"
    "ds_write_b128 %[ewr0], a[32:35] offset:4096\n\t"
    "ds_write_b128 %[ewr1], a[36:39] offset:5120\n\t"
    "ds_write_b128 %[ewr0], a[40:43] offset:6144\n\t"
    "ds_write_b128 %[ewr1], a[44:47] offset:7168\n\t"
    "ds_read_b64_tr_b16 a[64:65], %[trd0]\n\t"
    "ds_read_b64_tr_b16 a[66:67], %[trd0] offset:512\n\t"
    "ds_read_b64_tr_b16 a[68:69], %[trd0] offset:4096\n\t"
    "ds_read_b64_tr_b16 a[70:71], %[trd0] offset:4608\n\t"
    "ds_read_b64_tr_b16 a[72:73], %[trd1]\n\t"
    "ds_read_b64_tr_b16 a[74:75], %[trd1] offset:512\n\t"
    "ds_read_b64_tr_b16 a[76:77], %[trd1] offset:4096\n\t"
    "ds_read_b64_tr_b16 a[78:79], %[trd1] offset:4608\n\t"
    "v_perm_b32 v96, v196, v192, %[sel0]\n\t"
    "v_perm_b32 v97, v204, v200, %[sel0]\n\t"
    "v_perm_b32 v98, v212, v208, %[sel0]\n\t"
    "v_perm_b32 v99, v220, v216, %[sel0]\n\t"
    "v_perm_b32 v100, v196, v192, %[sel1]\n\t"
    "v_perm_b32 v101, v204, v200, %[sel1]\n\t"
    "v_perm_b32 v102, v212, v208, %[sel1]\n\t"
    "v_perm_b32 v103, v220, v216, %[sel1]\n\t"
    "v_perm_b32 v104, v197, v193, %[sel0]\n\t"
    "v_perm_b32 v105, v205, v201, %[sel0]\n\t"
    "v_perm_b32 v106, v213, v209, %[sel0]\n\t"
    "v_perm_b32 v107, v221, v217, %[sel0]\n\t"
    "v_perm_b32 v108, v197, v193, %[sel1]\n\t"
    "v_perm_b32 v109, v205, v201, %[sel1]\n\t"
    "v_perm_b32 v110, v213, v209, %[sel1]\n\t"
    "v_perm_b32 v111, v221, v217, %[sel1]\n\t"
    "s_waitcnt lgkmcnt(0)\n\t"
    "v_mfma_f32_16x16x32_bf16 v[52:55], a[64:67], v[40:43], 0\n\t"
    "v_mfma_f32_16x16x32_bf16 v[56:59], a[72:75], v[40:43], 0\n\t"
    "v_mfma_f32_16x16x32_bf16 v[52:55], a[68:71], v[44:47], v[52:55]\n\t"
    "v_mfma_f32_16x16x32_bf16 v[56:59], a[76:79], v[44:47], v[56:59]\n\t"
    "v_perm_b32 v112, v198, v194, %[sel0]\n\t"
    "v_perm_b32 v113, v206, v202, %[sel0]\n\t"
    "v_perm_b32 v114, v214, v210, %[sel0]\n\t"
    "v_perm_b32 v115, v222, v218, %[sel0]\n\t"
    "v_perm_b32 v116, v198, v194, %[sel1]\n\t"
    "v_perm_b32 v117, v206, v202, %[sel1]\n\t"
    "v_perm_b32 v118, v214, v210, %[sel1]\n\t"
    "v_perm_b32 v119, v222, v218, %[sel1]\n\t"
    "v_perm_b32 v120, v199, v195, %[sel0]\n\t"
    "v_perm_b32 v121, v207, v203, %[sel0]\n\t"
    "v_perm_b32 v122, v215, v211, %[sel0]\n\t"
    "v_perm_b32 v123, v223, v219, %[sel0]\n\t"
    "v_perm_b32 v124, v199, v195, %[sel1]\n\t"
    "v_perm_b32 v125, v207, v203, %[sel1]\n\t"
    "v_perm_b32 v126, v215, v211, %[sel1]\n\t"
    "v_perm_b32 v127, v223, v219, %[sel1]\n\t"
    "v_cvt_pk_bf16_f32 v60, v52, v53\n\t"
    "v_cvt_pk_bf16_f32 v61, v54, v55\n\t"
    "v_cvt_pk_bf16_f32 v62, v56, v57\n\t"
    "v_cvt_pk_bf16_f32 v63, v58, v59\n\t"
    "s_nop 1\n\t"
    "v_mfma_f32_16x16x32_bf16 v[64:67], v[60:63], v[96:99], v[64:67]\n\t"
    "v_mfma_f32_16x16x32_bf16 v[68:71], v[60:63], v[100:103], v[68:71]\n\t"
    "v_mfma_f32_16x16x32_bf16 v[72:75], v[60:63], v[104:107], v[72:75]\n\t"
    "v_mfma_f32_16x16x32_bf16 v[76:79], v[60:63], v[108:111], v[76:79]\n\t"
    "v_mfma_f32_16x16x32_bf16 v[80:83], v[60:63], v[112:115], v[80:83]\n\t"
    "v_mfma_f32_16x16x32_bf16 v[84:87], v[60:63], v[116:119], v[84:87]\n\t"
    "v_mfma_f32_16x16x32_bf16 v[88:91], v[60:63], v[120:123], v[88:91]\n\t"
    "v_mfma_f32_16x16x32_bf16 v[92:95], v[60:63], v[124:127], v[92:95]\n\t"
    "buffer_load_dwordx4 v[192:195], %[vo0], %[rkv], %[so2] offen nt\n\t"
    "buffer_load_dwordx4 v[196:199], %[vo0], %[rkv], %[so2] offen offset:256 nt\n\t"
    "buffer_load_dwordx4 a[32:35], %[ef0], %[re], %[se2] offen\n\t"
    "buffer_load_dwordx4 v[200:203], %[vo0], %[rkv], %[so2] offen offset:512 nt\n\t"
    "buffer_load_dwordx4 v[204:207], %[vo0], %[rkv], %[so2] offen offset:768 nt\n\t"
    "buffer_load_dwordx4 a[36:39], %[ef1], %[re], %[se2] offen\n\t"
    "buffer_load_dwordx4 v[208:211], %[vo1], %[rkv], %[so2] offen nt\n\t"
    "buffer_load_dwordx4 v[212:215], %[vo1], %[rkv], %[so2] offen offset:256 nt\n\t"
    "buffer_load_dwordx4 a[40:43], %[ef2], %[re], %[se2] offen\n\t"
    "buffer_load_dwordx4 v[216:219], %[vo1], %[rkv], %[so2] offen offset:512 nt\n\t"
    "buffer_load_dwordx4 v[220:223], %[vo1], %[rkv], %[so2] offen offset:768 nt\n\t"
    "buffer_load_dwordx4 a[44:47], %[ef3], %[re], %[se2] offen\n\t"
    "ds_read_b64_tr_b16 a[64:65], %[trd2]\n\t"
    "ds_read_b64_tr_b16 a[66:67], %[trd2] offset:512\n\t"
    "ds_read_b64_tr_b16 a[68:69], %[trd2] offset:4096\n\t"
    "ds_read_b64_tr_b16 a[70:71], %[trd2] offset:4608\n\t"
    "ds_read_b64_tr_b16 a[72:73], %[trd3]\n\t"
    "ds_read_b64_tr_b16 a[74:75], %[trd3] offset:512\n\t"
    "ds_read_b64_tr_b16 a[76:77], %[trd3] offset:4096\n\t"
    "ds_read_b64_tr_b16 a[78:79], %[trd3] offset:4608\n\t"
    "s_waitcnt lgkmcnt(0)\n\t"
    "v_mfma_f32_16x16x32_bf16 v[52:55], a[64:67], v[40:43], 0\n\t"
    "v_mfma_f32_16x16x32_bf16 v[56:59], a[72:75], v[40:43], 0\n\t"
    "v_mfma_f32_16x16x32_bf16 v[52:55], a[68:71], v[44:47], v[52:55]\n\t"
    "v_mfma_f32_16x16x32_bf16 v[56:59], a[76:79], v[44:47], v[56:59]\n\t"
    "s_nop 7\n\t"
    "v_cvt_pk_bf16_f32 v60, v52, v53\n\t"
    "v_cvt_pk_bf16_f32 v61, v54, v55\n\t"
    "v_cvt_pk_bf16_f32 v62, v56, v57\n\t"
    "v_cvt_pk_bf16_f32 v63, v58, v59\n\t"
    "s_waitcnt vmcnt(36)\n\t"
    "ds_write_b128 %[ewr0], a[48:51]\n\t"
    "ds_write_b128 %[ewr1], a[52:55] offset:1024\n\t"
    "ds_write_b128 %[ewr0], a[56:59] offset:2048\n\t"
    "ds_write_b128 %[ewr1], a[60:63] offset:3072\n\t"
    "v_perm_b32 v96, v228, v224, %[sel0]\n\t"
    "v_perm_b32 v97, v236, v232, %[sel0]\n\t"
    "v_perm_b32 v98, v244, v240, %[sel0]\n\t"
    "v_perm_b32 v99, v252, v248, %[sel0]\n\t"
    "v_perm_b32 v100, v228, v224, %[sel1]\n\t"
    "v_perm_b32 v101, v236, v232, %[sel1]\n\t"
    "v_perm_b32 v102, v244, v240, %[sel1]\n\t"
    "v_perm_b32 v103, v252, v248, %[sel1]\n\t"
    "v_perm_b32 v104, v229, v225, %[sel0]\n\t"
    "v_perm_b32 v105, v237, v233, %[sel0]\n\t"
    "v_perm_b32 v106, v245, v241, %[sel0]\n\t"
    "v_perm_b32 v107, v253, v249, %[sel0]\n\t"
    "v_perm_b32 v108, v229, v225, %[sel1]\n\t"
    "v_perm_b32 v109, v237, v233, %[sel1]\n\t"
    "v_perm_b32 v110, v245, v241, %[sel1]\n\t"
    "v_perm_b32 v111, v253, v249, %[sel1]\n\t"
    "v_mfma_f32_16x16x32_bf16 v[64:67], v[60:63], v[96:99], v[64:67]\n\t"
    "v_mfma_f32_16x16x32_bf16 v[68:71], v[60:63], v[100:103], v[68:71]\n\t"
    "v_perm_b32 v112, v230, v226, %[sel0]\n\t"
    "v_perm_b32 v113, v238, v234, %[sel0]\n\t"
    "v_perm_b32 v114, v246, v242, %[sel0]\n\t"
    "v_perm_b32 v115, v254, v250, %[sel0]\n\t"
    "v_mfma_f32_16x16x32_bf16 v[72:75], v[60:63], v[104:107], v[72:75]\n\t"
    "v_perm_b32 v116, v230, v226, %[sel1]\n\t"
    "v_perm_b32 v117, v238, v234, %[sel1]\n\t"
    "v_perm_b32 v118, v246, v242, %[sel1]\n\t"
    "v_perm_b32 v119, v254, v250, %[sel1]\n\t"
    "v_mfma_f32_16x16x32_bf16 v[76:79], v[60:63], v[108:111], v[76:79]\n\t"
    "v_perm_b32 v120, v231, v227, %[sel0]\n\t"
    "v_perm_b32 v121, v239, v235, %[sel0]\n\t"
    "v_perm_b32 v122, v247, v243, %[sel0]\n\t"
    "v_perm_b32 v123, v255, v251, %[sel0]\n\t"
    "v_mfma_f32_16x16x32_bf16 v[80:83], v[60:63], v[112:115], v[80:83]\n\t"
    "v_perm_b32 v124, v231, v227, %[sel1]\n\t"
    "v_perm_b32 v125, v239, v235, %[sel1]\n\t"
    "v_perm_b32 v126, v247, v243, %[sel1]\n\t"
    "v_perm_b32 v127, v255, v251, %[sel1]\n\t"
    "v_mfma_f32_16x16x32_bf16 v[84:87], v[60:63], v[116:119], v[84:87]\n\t"
    "v_mfma_f32_16x16x32_bf16 v[88:91], v[60:63], v[120:123], v[88:91]\n\t"
    "v_mfma_f32_16x16x32_bf16 v[92:95], v[60:63], v[124:127], v[92:95]\n\t"
    "s_waitcnt lgkmcnt(0)\n\t"
    "buffer_load_dwordx4 v[224:227], %[vo0], %[rkv], %[so3] offen nt\n\t"
    "buffer_load_dwordx4 v[228:231], %[vo0], %[rkv], %[so3] offen offset:256 nt\n\t"
    "buffer_load_dwordx4 a[48:51], %[ef0], %[re4], %[se3] offen\n\t"
    "buffer_load_dwordx4 v[232:235], %[vo0], %[rkv], %[so3] offen offset:512 nt\n\t"
    "buffer_load_dwordx4 v[236:239], %[vo0], %[rkv], %[so3] offen offset:768 nt\n\t"
    "buffer_load_dwordx4 a[52:55], %[ef1], %[re4], %[se3] offen\n\t"
    "buffer_load_dwordx4 v[240:243], %[vo1], %[rkv], %[so3] offen nt\n\t"
    "buffer_load_dwordx4 v[244:247], %[vo1], %[rkv], %[so3] offen offset:256 nt\n\t"
    "buffer_load_dwordx4 a[56:59], %[ef2], %[re4], %[se3] offen\n\t"
    "buffer_load_dwordx4 v[248:251], %[vo1], %[rkv], %[so3] offen offset:512 nt\n\t"
    "buffer_load_dwordx4 v[252:255], %[vo1], %[rkv], %[so3] offen offset:768 nt\n\t"
    "buffer_load_dwordx4 a[60:63], %[ef3], %[re4], %[se3] offen\n\t"
        : RING_OPS, "+{v[64:95]}"(oacc)
        : LOAD_OPS, V_OPS
        : "memory", T_CLOB);
  }
  asm volatile(
    "s_waitcnt vmcnt(36)\n\t"
    "ds_write_b128 %[ewr0], a[0:3] offset:4096\n\t"
    "ds_write_b128 %[ewr1], a[4:7] offset:5120\n\t"
    "ds_write_b128 %[ewr0], a[8:11] offset:6144\n\t"
    "ds_write_b128 %[ewr1], a[12:15] offset:7168\n\t"
    "ds_read_b64_tr_b16 a[64:65], %[trd0]\n\t"
    "ds_read_b64_tr_b16 a[66:67], %[trd0] offset:512\n\t"
    "ds_read_b64_tr_b16 a[68:69], %[trd0] offset:4096\n\t"
    "ds_read_b64_tr_b16 a[70:71], %[trd0] offset:4608\n\t"
    "ds_read_b64_tr_b16 a[72:73], %[trd1]\n\t"
    "ds_read_b64_tr_b16 a[74:75], %[trd1] offset:512\n\t"
    "ds_read_b64_tr_b16 a[76:77], %[trd1] offset:4096\n\t"
    "ds_read_b64_tr_b16 a[78:79], %[trd1] offset:4608\n\t"
    "v_perm_b32 v96, v132, v128, %[sel0]\n\t"
    "v_perm_b32 v97, v140, v136, %[sel0]\n\t"
    "v_perm_b32 v98, v148, v144, %[sel0]\n\t"
    "v_perm_b32 v99, v156, v152, %[sel0]\n\t"
    "v_perm_b32 v100, v132, v128, %[sel1]\n\t"
    "v_perm_b32 v101, v140, v136, %[sel1]\n\t"
    "v_perm_b32 v102, v148, v144, %[sel1]\n\t"
    "v_perm_b32 v103, v156, v152, %[sel1]\n\t"
    "v_perm_b32 v104, v133, v129, %[sel0]\n\t"
    "v_perm_b32 v105, v141, v137, %[sel0]\n\t"
    "v_perm_b32 v106, v149, v145, %[sel0]\n\t"
    "v_perm_b32 v107, v157, v153, %[sel0]\n\t"
    "v_perm_b32 v108, v133, v129, %[sel1]\n\t"
    "v_perm_b32 v109, v141, v137, %[sel1]\n\t"
    "v_perm_b32 v110, v149, v145, %[sel1]\n\t"
    "v_perm_b32 v111, v157, v153, %[sel1]\n\t"
    "s_waitcnt lgkmcnt(0)\n\t"
    "v_mfma_f32_16x16x32_bf16 v[52:55], a[64:67], v[40:43], 0\n\t"
    "v_mfma_f32_16x16x32_bf16 v[56:59], a[72:75], v[40:43], 0\n\t"
    "v_mfma_f32_16x16x32_bf16 v[52:55], a[68:71], v[44:47], v[52:55]\n\t"
    "v_mfma_f32_16x16x32_bf16 v[56:59], a[76:79], v[44:47], v[56:59]\n\t"
    "v_perm_b32 v112, v134, v130, %[sel0]\n\t"
    "v_perm_b32 v113, v142, v138, %[sel0]\n\t"
    "v_perm_b32 v114, v150, v146, %[sel0]\n\t"
    "v_perm_b32 v115, v158, v154, %[sel0]\n\t"
    "v_perm_b32 v116, v134, v130, %[sel1]\n\t"
    "v_perm_b32 v117, v142, v138, %[sel1]\n\t"
    "v_perm_b32 v118, v150, v146, %[sel1]\n\t"
    "v_perm_b32 v119, v158, v154, %[sel1]\n\t"
    "v_perm_b32 v120, v135, v131, %[sel0]\n\t"
    "v_perm_b32 v121, v143, v139, %[sel0]\n\t"
    "v_perm_b32 v122, v151, v147, %[sel0]\n\t"
    "v_perm_b32 v123, v159, v155, %[sel0]\n\t"
    "v_perm_b32 v124, v135, v131, %[sel1]\n\t"
    "v_perm_b32 v125, v143, v139, %[sel1]\n\t"
    "v_perm_b32 v126, v151, v147, %[sel1]\n\t"
    "v_perm_b32 v127, v159, v155, %[sel1]\n\t"
    "v_cvt_pk_bf16_f32 v60, v52, v53\n\t"
    "v_cvt_pk_bf16_f32 v61, v54, v55\n\t"
    "v_cvt_pk_bf16_f32 v62, v56, v57\n\t"
    "v_cvt_pk_bf16_f32 v63, v58, v59\n\t"
    "s_nop 1\n\t"
    "v_mfma_f32_16x16x32_bf16 v[64:67], v[60:63], v[96:99], v[64:67]\n\t"
    "v_mfma_f32_16x16x32_bf16 v[68:71], v[60:63], v[100:103], v[68:71]\n\t"
    "v_mfma_f32_16x16x32_bf16 v[72:75], v[60:63], v[104:107], v[72:75]\n\t"
    "v_mfma_f32_16x16x32_bf16 v[76:79], v[60:63], v[108:111], v[76:79]\n\t"
    "v_mfma_f32_16x16x32_bf16 v[80:83], v[60:63], v[112:115], v[80:83]\n\t"
    "v_mfma_f32_16x16x32_bf16 v[84:87], v[60:63], v[116:119], v[84:87]\n\t"
    "v_mfma_f32_16x16x32_bf16 v[88:91], v[60:63], v[120:123], v[88:91]\n\t"
    "v_mfma_f32_16x16x32_bf16 v[92:95], v[60:63], v[124:127], v[92:95]\n\t"
    "ds_read_b64_tr_b16 a[64:65], %[trd2]\n\t"
    "ds_read_b64_tr_b16 a[66:67], %[trd2] offset:512\n\t"
    "ds_read_b64_tr_b16 a[68:69], %[trd2] offset:4096\n\t"
    "ds_read_b64_tr_b16 a[70:71], %[trd2] offset:4608\n\t"
    "ds_read_b64_tr_b16 a[72:73], %[trd3]\n\t"
    "ds_read_b64_tr_b16 a[74:75], %[trd3] offset:512\n\t"
    "ds_read_b64_tr_b16 a[76:77], %[trd3] offset:4096\n\t"
    "ds_read_b64_tr_b16 a[78:79], %[trd3] offset:4608\n\t"
    "s_waitcnt lgkmcnt(0)\n\t"
    "v_mfma_f32_16x16x32_bf16 v[52:55], a[64:67], v[40:43], 0\n\t"
    "v_mfma_f32_16x16x32_bf16 v[56:59], a[72:75], v[40:43], 0\n\t"
    "v_mfma_f32_16x16x32_bf16 v[52:55], a[68:71], v[44:47], v[52:55]\n\t"
    "v_mfma_f32_16x16x32_bf16 v[56:59], a[76:79], v[44:47], v[56:59]\n\t"
    "s_nop 7\n\t"
    "v_cvt_pk_bf16_f32 v60, v52, v53\n\t"
    "v_cvt_pk_bf16_f32 v61, v54, v55\n\t"
    "v_cvt_pk_bf16_f32 v62, v56, v57\n\t"
    "v_cvt_pk_bf16_f32 v63, v58, v59\n\t"
    "s_waitcnt vmcnt(24)\n\t"
    "ds_write_b128 %[ewr0], a[16:19]\n\t"
    "ds_write_b128 %[ewr1], a[20:23] offset:1024\n\t"
    "ds_write_b128 %[ewr0], a[24:27] offset:2048\n\t"
    "ds_write_b128 %[ewr1], a[28:31] offset:3072\n\t"
    "v_perm_b32 v96, v164, v160, %[sel0]\n\t"
    "v_perm_b32 v97, v172, v168, %[sel0]\n\t"
    "v_perm_b32 v98, v180, v176, %[sel0]\n\t"
    "v_perm_b32 v99, v188, v184, %[sel0]\n\t"
    "v_perm_b32 v100, v164, v160, %[sel1]\n\t"
    "v_perm_b32 v101, v172, v168, %[sel1]\n\t"
    "v_perm_b32 v102, v180, v176, %[sel1]\n\t"
    "v_perm_b32 v103, v188, v184, %[sel1]\n\t"
    "v_perm_b32 v104, v165, v161, %[sel0]\n\t"
    "v_perm_b32 v105, v173, v169, %[sel0]\n\t"
    "v_perm_b32 v106, v181, v177, %[sel0]\n\t"
    "v_perm_b32 v107, v189, v185, %[sel0]\n\t"
    "v_perm_b32 v108, v165, v161, %[sel1]\n\t"
    "v_perm_b32 v109, v173, v169, %[sel1]\n\t"
    "v_perm_b32 v110, v181, v177, %[sel1]\n\t"
    "v_perm_b32 v111, v189, v185, %[sel1]\n\t"
    "v_mfma_f32_16x16x32_bf16 v[64:67], v[60:63], v[96:99], v[64:67]\n\t"
    "v_mfma_f32_16x16x32_bf16 v[68:71], v[60:63], v[100:103], v[68:71]\n\t"
    "v_perm_b32 v112, v166, v162, %[sel0]\n\t"
    "v_perm_b32 v113, v174, v170, %[sel0]\n\t"
    "v_perm_b32 v114, v182, v178, %[sel0]\n\t"
    "v_perm_b32 v115, v190, v186, %[sel0]\n\t"
    "v_mfma_f32_16x16x32_bf16 v[72:75], v[60:63], v[104:107], v[72:75]\n\t"
    "v_perm_b32 v116, v166, v162, %[sel1]\n\t"
    "v_perm_b32 v117, v174, v170, %[sel1]\n\t"
    "v_perm_b32 v118, v182, v178, %[sel1]\n\t"
    "v_perm_b32 v119, v190, v186, %[sel1]\n\t"
    "v_mfma_f32_16x16x32_bf16 v[76:79], v[60:63], v[108:111], v[76:79]\n\t"
    "v_perm_b32 v120, v167, v163, %[sel0]\n\t"
    "v_perm_b32 v121, v175, v171, %[sel0]\n\t"
    "v_perm_b32 v122, v183, v179, %[sel0]\n\t"
    "v_perm_b32 v123, v191, v187, %[sel0]\n\t"
    "v_mfma_f32_16x16x32_bf16 v[80:83], v[60:63], v[112:115], v[80:83]\n\t"
    "v_perm_b32 v124, v167, v163, %[sel1]\n\t"
    "v_perm_b32 v125, v175, v171, %[sel1]\n\t"
    "v_perm_b32 v126, v183, v179, %[sel1]\n\t"
    "v_perm_b32 v127, v191, v187, %[sel1]\n\t"
    "v_mfma_f32_16x16x32_bf16 v[84:87], v[60:63], v[116:119], v[84:87]\n\t"
    "v_mfma_f32_16x16x32_bf16 v[88:91], v[60:63], v[120:123], v[88:91]\n\t"
    "v_mfma_f32_16x16x32_bf16 v[92:95], v[60:63], v[124:127], v[92:95]\n\t"
    "s_waitcnt vmcnt(12)\n\t"
    "ds_write_b128 %[ewr0], a[32:35] offset:4096\n\t"
    "ds_write_b128 %[ewr1], a[36:39] offset:5120\n\t"
    "ds_write_b128 %[ewr0], a[40:43] offset:6144\n\t"
    "ds_write_b128 %[ewr1], a[44:47] offset:7168\n\t"
    "ds_read_b64_tr_b16 a[64:65], %[trd0]\n\t"
    "ds_read_b64_tr_b16 a[66:67], %[trd0] offset:512\n\t"
    "ds_read_b64_tr_b16 a[68:69], %[trd0] offset:4096\n\t"
    "ds_read_b64_tr_b16 a[70:71], %[trd0] offset:4608\n\t"
    "ds_read_b64_tr_b16 a[72:73], %[trd1]\n\t"
    "ds_read_b64_tr_b16 a[74:75], %[trd1] offset:512\n\t"
    "ds_read_b64_tr_b16 a[76:77], %[trd1] offset:4096\n\t"
    "ds_read_b64_tr_b16 a[78:79], %[trd1] offset:4608\n\t"
    "v_perm_b32 v96, v196, v192, %[sel0]\n\t"
    "v_perm_b32 v97, v204, v200, %[sel0]\n\t"
    "v_perm_b32 v98, v212, v208, %[sel0]\n\t"
    "v_perm_b32 v99, v220, v216, %[sel0]\n\t"
    "v_perm_b32 v100, v196, v192, %[sel1]\n\t"
    "v_perm_b32 v101, v204, v200, %[sel1]\n\t"
    "v_perm_b32 v102, v212, v208, %[sel1]\n\t"
    "v_perm_b32 v103, v220, v216, %[sel1]\n\t"
    "v_perm_b32 v104, v197, v193, %[sel0]\n\t"
    "v_perm_b32 v105, v205, v201, %[sel0]\n\t"
    "v_perm_b32 v106, v213, v209, %[sel0]\n\t"
    "v_perm_b32 v107, v221, v217, %[sel0]\n\t"
    "v_perm_b32 v108, v197, v193, %[sel1]\n\t"
    "v_perm_b32 v109, v205, v201, %[sel1]\n\t"
    "v_perm_b32 v110, v213, v209, %[sel1]\n\t"
    "v_perm_b32 v111, v221, v217, %[sel1]\n\t"
    "s_waitcnt lgkmcnt(0)\n\t"
    "v_mfma_f32_16x16x32_bf16 v[52:55], a[64:67], v[40:43], 0\n\t"
    "v_mfma_f32_16x16x32_bf16 v[56:59], a[72:75], v[40:43], 0\n\t"
    "v_mfma_f32_16x16x32_bf16 v[52:55], a[68:71], v[44:47], v[52:55]\n\t"
    "v_mfma_f32_16x16x32_bf16 v[56:59], a[76:79], v[44:47], v[56:59]\n\t"
    "v_perm_b32 v112, v198, v194, %[sel0]\n\t"
    "v_perm_b32 v113, v206, v202, %[sel0]\n\t"
    "v_perm_b32 v114, v214, v210, %[sel0]\n\t"
    "v_perm_b32 v115, v222, v218, %[sel0]\n\t"
    "v_perm_b32 v116, v198, v194, %[sel1]\n\t"
    "v_perm_b32 v117, v206, v202, %[sel1]\n\t"
    "v_perm_b32 v118, v214, v210, %[sel1]\n\t"
    "v_perm_b32 v119, v222, v218, %[sel1]\n\t"
    "v_perm_b32 v120, v199, v195, %[sel0]\n\t"
    "v_perm_b32 v121, v207, v203, %[sel0]\n\t"
    "v_perm_b32 v122, v215, v211, %[sel0]\n\t"
    "v_perm_b32 v123, v223, v219, %[sel0]\n\t"
    "v_perm_b32 v124, v199, v195, %[sel1]\n\t"
    "v_perm_b32 v125, v207, v203, %[sel1]\n\t"
    "v_perm_b32 v126, v215, v211, %[sel1]\n\t"
    "v_perm_b32 v127, v223, v219, %[sel1]\n\t"
    "v_cvt_pk_bf16_f32 v60, v52, v53\n\t"
    "v_cvt_pk_bf16_f32 v61, v54, v55\n\t"
    "v_cvt_pk_bf16_f32 v62, v56, v57\n\t"
    "v_cvt_pk_bf16_f32 v63, v58, v59\n\t"
    "s_nop 1\n\t"
    "v_mfma_f32_16x16x32_bf16 v[64:67], v[60:63], v[96:99], v[64:67]\n\t"
    "v_mfma_f32_16x16x32_bf16 v[68:71], v[60:63], v[100:103], v[68:71]\n\t"
    "v_mfma_f32_16x16x32_bf16 v[72:75], v[60:63], v[104:107], v[72:75]\n\t"
    "v_mfma_f32_16x16x32_bf16 v[76:79], v[60:63], v[108:111], v[76:79]\n\t"
    "v_mfma_f32_16x16x32_bf16 v[80:83], v[60:63], v[112:115], v[80:83]\n\t"
    "v_mfma_f32_16x16x32_bf16 v[84:87], v[60:63], v[116:119], v[84:87]\n\t"
    "v_mfma_f32_16x16x32_bf16 v[88:91], v[60:63], v[120:123], v[88:91]\n\t"
    "v_mfma_f32_16x16x32_bf16 v[92:95], v[60:63], v[124:127], v[92:95]\n\t"
    "ds_read_b64_tr_b16 a[64:65], %[trd2]\n\t"
    "ds_read_b64_tr_b16 a[66:67], %[trd2] offset:512\n\t"
    "ds_read_b64_tr_b16 a[68:69], %[trd2] offset:4096\n\t"
    "ds_read_b64_tr_b16 a[70:71], %[trd2] offset:4608\n\t"
    "ds_read_b64_tr_b16 a[72:73], %[trd3]\n\t"
    "ds_read_b64_tr_b16 a[74:75], %[trd3] offset:512\n\t"
    "ds_read_b64_tr_b16 a[76:77], %[trd3] offset:4096\n\t"
    "ds_read_b64_tr_b16 a[78:79], %[trd3] offset:4608\n\t"
    "s_waitcnt lgkmcnt(0)\n\t"
    "v_mfma_f32_16x16x32_bf16 v[52:55], a[64:67], v[40:43], 0\n\t"
    "v_mfma_f32_16x16x32_bf16 v[56:59], a[72:75], v[40:43], 0\n\t"
    "v_mfma_f32_16x16x32_bf16 v[52:55], a[68:71], v[44:47], v[52:55]\n\t"
    "v_mfma_f32_16x16x32_bf16 v[56:59], a[76:79], v[44:47], v[56:59]\n\t"
    "s_nop 7\n\t"
    "v_cvt_pk_bf16_f32 v60, v52, v53\n\t"
    "v_cvt_pk_bf16_f32 v61, v54, v55\n\t"
    "v_cvt_pk_bf16_f32 v62, v56, v57\n\t"
    "v_cvt_pk_bf16_f32 v63, v58, v59\n\t"
    "s_waitcnt vmcnt(0)\n\t"
    "v_perm_b32 v96, v228, v224, %[sel0]\n\t"
    "v_perm_b32 v97, v236, v232, %[sel0]\n\t"
    "v_perm_b32 v98, v244, v240, %[sel0]\n\t"
    "v_perm_b32 v99, v252, v248, %[sel0]\n\t"
    "v_perm_b32 v100, v228, v224, %[sel1]\n\t"
    "v_perm_b32 v101, v236, v232, %[sel1]\n\t"
    "v_perm_b32 v102, v244, v240, %[sel1]\n\t"
    "v_perm_b32 v103, v252, v248, %[sel1]\n\t"
    "v_perm_b32 v104, v229, v225, %[sel0]\n\t"
    "v_perm_b32 v105, v237, v233, %[sel0]\n\t"
    "v_perm_b32 v106, v245, v241, %[sel0]\n\t"
    "v_perm_b32 v107, v253, v249, %[sel0]\n\t"
    "v_perm_b32 v108, v229, v225, %[sel1]\n\t"
    "v_perm_b32 v109, v237, v233, %[sel1]\n\t"
    "v_perm_b32 v110, v245, v241, %[sel1]\n\t"
    "v_perm_b32 v111, v253, v249, %[sel1]\n\t"
    "v_mfma_f32_16x16x32_bf16 v[64:67], v[60:63], v[96:99], v[64:67]\n\t"
    "v_mfma_f32_16x16x32_bf16 v[68:71], v[60:63], v[100:103], v[68:71]\n\t"
    "v_perm_b32 v112, v230, v226, %[sel0]\n\t"
    "v_perm_b32 v113, v238, v234, %[sel0]\n\t"
    "v_perm_b32 v114, v246, v242, %[sel0]\n\t"
    "v_perm_b32 v115, v254, v250, %[sel0]\n\t"
    "v_mfma_f32_16x16x32_bf16 v[72:75], v[60:63], v[104:107], v[72:75]\n\t"
    "v_perm_b32 v116, v230, v226, %[sel1]\n\t"
    "v_perm_b32 v117, v238, v234, %[sel1]\n\t"
    "v_perm_b32 v118, v246, v242, %[sel1]\n\t"
    "v_perm_b32 v119, v254, v250, %[sel1]\n\t"
    "v_mfma_f32_16x16x32_bf16 v[76:79], v[60:63], v[108:111], v[76:79]\n\t"
    "v_perm_b32 v120, v231, v227, %[sel0]\n\t"
    "v_perm_b32 v121, v239, v235, %[sel0]\n\t"
    "v_perm_b32 v122, v247, v243, %[sel0]\n\t"
    "v_perm_b32 v123, v255, v251, %[sel0]\n\t"
    "v_mfma_f32_16x16x32_bf16 v[80:83], v[60:63], v[112:115], v[80:83]\n\t"
    "v_perm_b32 v124, v231, v227, %[sel1]\n\t"
    "v_perm_b32 v125, v239, v235, %[sel1]\n\t"
    "v_perm_b32 v126, v247, v243, %[sel1]\n\t"
    "v_perm_b32 v127, v255, v251, %[sel1]\n\t"
    "v_mfma_f32_16x16x32_bf16 v[84:87], v[60:63], v[116:119], v[84:87]\n\t"
    "v_mfma_f32_16x16x32_bf16 v[88:91], v[60:63], v[120:123], v[88:91]\n\t"
    "v_mfma_f32_16x16x32_bf16 v[92:95], v[60:63], v[124:127], v[92:95]\n\t"
      "s_nop 7"
      : RING_OPS, "+{v[64:95]}"(oacc)
      : V_OPS
      : "memory", T_CLOB);

#pragma unroll
  for (int cc = 0; cc < 8; ++cc) lds[L_O + w * 512 + cc * 64 + lane] = oacc[4 * cc];
  asm volatile("s_waitcnt lgkmcnt(0)" ::: "memory");
  __builtin_amdgcn_s_barrier();
  asm volatile("" ::: "memory");
  float s[4];
#pragma unroll
  for (int k = 0; k < 4; ++k) s[k] = lds[L_O + (4 * w + k) * 64 + lane] + lds[L_O + 512 + (4 * w + k) * 64 + lane];
  *(GAS u32x2*)((GAS char*)out_ + (size_t)head * (SQ * DH * 2) + lane * 16 + w * 8) = u32x2{cvtpk(s[0], s[1]), cvtpk(s[2], s[3])};
}

}

extern "C" void kernel_launch(void* const* d_in, const int* in_sizes, int n_in, void* d_out, int out_size,
                              void* d_ws, size_t ws_size, hipStream_t stream) {
  (void)in_sizes; (void)n_in; (void)out_size; (void)d_ws; (void)ws_size;
  lin::linformer_kernel<<<lin::NHEAD, lin::NW * 64, 0, stream>>>((const char*)d_in[0], (const char*)d_in[1],
                                                                 (const char*)d_in[2], (const char*)d_in[3],
                                                                 (const char*)d_in[4], (char*)d_out);
}
```
